```python
import torch

def _softmax(x, axis=-1):
    return torch.softmax(x, dim=axis)
def _gelu(x):
    return torch.nn.functional.gelu(x, approximate="tanh")
def _silu(x):
    return torch.nn.functional.silu(x)
def _sigmoid(x):
    return torch.sigmoid(x)
def _layernorm(x, axis=-1, eps=1e-5):
    m = x.mean(dim=axis, keepdim=True)
    v = x.var(dim=axis, keepdim=True, unbiased=False)
    return (x - m) / torch.sqrt(v + eps)
def _scatter_add(tgt, idx, src, axis=0):
    return tgt.clone().index_add_(axis, idx, src)
def _segment_sum(src, seg, n):
    out = torch.zeros((n,) + src.shape[1:], dtype=src.dtype)
    return out.index_add_(0, seg, src)
def _pad(x, pw):
    flat = tuple(v for pair in reversed(pw) for v in pair)
    return torch.nn.functional.pad(x, flat)

kernel_name = 'standalone__deformable_dcnv1__vision'

def setup_inputs():

    _w = (lambda a: (lambda t: t.to(torch.bfloat16) if t.is_floating_point() else t)(torch.as_tensor(a)))
    H = W = 224
    C = 96
    K = 5
    return {
        "x": _w((lambda *s: torch.randn(*s, dtype=torch.bfloat16) * 0.05)(1, H, W, C)),
        "offset": _w((lambda *s: torch.randn(*s, dtype=torch.bfloat16) * 0.05)(1, H, W, 2 * K * K) * 0.5),
        "w": _w((lambda *s: torch.randn(*s, dtype=torch.bfloat16) * 0.05)(K, K, C, C) * (K * K * C) ** -0.5),
    }

def reference(x, offset, w):
    B, H, W, C = x.shape
    K = 5
    hh = (lambda x, dt: x.to(dt))(torch.arange(H), x.dtype).reshape(1, H, 1)
    ww = (lambda x, dt: x.to(dt))(torch.arange(W), x.dtype).reshape(1, 1, W)
    xf = x.reshape(B, H * W, C)
    def _gather(yi, xi):
        lin = (yi * W + xi).reshape(B, H * W)
        idx = (lambda x, n, axis: torch.repeat_interleave(x, n, dim=axis))(lin[..., None], C, axis=-1)
        return (lambda x, i, axis: torch.take_along_dim(x, i, dim=axis))(xf, idx, axis=1).reshape(B, H, W, C)
    taps = []
    for ki in range(K):
        for kj in range(K):
            t = ki * K + kj
            dy = offset[..., 2 * t]
            dx = offset[..., 2 * t + 1]
            py = torch.clip(hh + (ki - 0.5 * (K - 1)) + dy, 0.0, H - 1.0)
            px = torch.clip(ww + (kj - 0.5 * (K - 1)) + dx, 0.0, W - 1.0)
            y0 = torch.floor(py)
            x0 = torch.floor(px)
            y1 = torch.clip(y0 + 1.0, 0.0, H - 1.0)
            x1 = torch.clip(x0 + 1.0, 0.0, W - 1.0)
            wy = (py - y0)[..., None]
            wx = (px - x0)[..., None]
            y0i = (lambda x, dt: x.to(dt))(y0, torch.int64)
            y1i = (lambda x, dt: x.to(dt))(y1, torch.int64)
            x0i = (lambda x, dt: x.to(dt))(x0, torch.int64)
            x1i = (lambda x, dt: x.to(dt))(x1, torch.int64)
            v = (1 - wy) * (1 - wx) * _gather(y0i, x0i) + (1 - wy) * wx * _gather(y0i, x1i) + wy * (1 - wx) * _gather(y1i, x0i) + wy * wx * _gather(y1i, x1i)
            taps.append(v)
    samp = torch.stack(taps, 3)
    return torch.einsum("bhwkc,kco->bhwo", samp, w.reshape(K * K, C, C))
```

```cpp
#include <hip/hip_runtime.h>
#include <stdint.h>

typedef uint32_t u32;
typedef uint64_t u64;
typedef __attribute__((ext_vector_type(2))) __bf16 bf2;
typedef __attribute__((ext_vector_type(8))) __bf16 bf8;
typedef __attribute__((ext_vector_type(4))) short s4;
typedef __attribute__((ext_vector_type(2))) float f2;
typedef __attribute__((ext_vector_type(4))) float f4;
typedef __attribute__((ext_vector_type(2))) u32 u32x2;
typedef __attribute__((ext_vector_type(4))) u32 u32x4;
typedef __attribute__((ext_vector_type(8))) u32 u32x8;
#define LDS(T) __attribute__((address_space(3))) T
#define LDSP(T, a) ((LDS(T)*)(size_t)(a))

constexpr int IMG = 224, PXB = 192, ROWB = IMG * PXB;
constexpr int STAGE = 18432, NS = 224;
constexpr int WRING = 384 * PXB, POOL = WRING + 2 * STAGE, CNT = POOL + NS * PXB + 4, LIST = CNT + 124;
constexpr int LSZ = NS * 12, IRR = CNT + 4 * 28, TBL = 161792, LDS_BYTES = TBL + 2048;
static_assert(3 * LSZ + 128 == 8192 && LIST + 3 * LSZ == TBL && LDS_BYTES == 163840, "lds");


#define RESTART_IN [wv] "s"(wv), [ka] "s"(ka), [bid] "s"(bid), [re] "s"(re)
#define BARRIER(waits) asm volatile("s_waitcnt " waits "\n\ts_barrier" ::: "memory")

__device__ __forceinline__ u32x4 make_rsrc(u32 lo, u32 hi, u32 bytes) { return u32x4{lo, hi & 0xffffu, bytes, 0x00020000u}; }
__device__ __forceinline__ u32 lane_id() {
    u32 l;
    asm volatile("v_mbcnt_lo_u32_b32 %0, -1, 0\n\tv_mbcnt_hi_u32_b32 %0, -1, %0" : "=v"(l));
    __builtin_assume(l < 64);
    return l;
}
__device__ __forceinline__ int to_sgpr(int v) {
    int r;
    asm("s_nop 1\n\tv_readfirstlane_b32 %0, %1\n\ts_nop 4" : "=s"(r) : "v"(v));
    return r;
}
#define LANE_VARS const u32 lane = lane_id(), p = lane & 15, q = lane >> 4
#define WB (WRING + (8 * q + (p >> 2)) * PXB + (p & 3) * 8 + (q & 1) * 32)
__device__ __forceinline__ u32 pk_bf16(float a, float b) {
    f2 v = {a, b};
    return __builtin_bit_cast(u32, __builtin_convertvector(v, bf2));
}
__device__ __forceinline__ float lo_f(u32 v) { return __uint_as_float(v << 16); }
__device__ __forceinline__ float hi_f(u32 v) { return __uint_as_float(v & 0xffff0000u); }
__device__ __forceinline__ s4 tr_read(u32 a) { return __builtin_amdgcn_ds_read_tr16_b64_v4i16(LDSP(s4, a)); }

struct BatchCtx { u32 l4, c8, sel0, sel1, sel2, sel3, col, pitch; };

__device__ __forceinline__ BatchCtx batch_ctx(u32 lane, u32 pitch) {
    BatchCtx c;
    u32 p = lane & 15, q = lane >> 4, a = (lane >> 2) & 3;
    c.l4 = lane >> 2; c.c8 = (lane & 3) * 8; c.col = 4 * (p & 3) + (p >> 2); c.pitch = pitch;
    u32 s0 = (a & 1) ? 0x01000c0cu : 0x0c0c0100u, s1 = (a & 1) ? 0x03020c0cu : 0x0c0c0302u;
    bool lo = (lane & 3) == q && a < 2, hi = (lane & 3) == q && a >= 2;
    c.sel0 = lo ? s0 : 0x0c0c0c0cu; c.sel1 = hi ? s0 : 0x0c0c0c0cu;
    c.sel2 = lo ? s1 : 0x0c0c0c0cu; c.sel3 = hi ? s1 : 0x0c0c0c0cu;
    return c;
}

__device__ __forceinline__ void interp_pass(f4 (&acc)[6], u32 aA, u32 aB, u32 w, const BatchCtx& c) {
    u32x4 bw = {__builtin_amdgcn_perm(w, w, c.sel0), __builtin_amdgcn_perm(w, w, c.sel1),
                __builtin_amdgcn_perm(w, w, c.sel2), __builtin_amdgcn_perm(w, w, c.sel3)};
    s4 ga[6], gb[6];
#pragma unroll
    for (int i = 0; i < 6; ++i) {
        ga[i] = tr_read(aA + 64 * (i >> 1) + 8 * (i & 1));
        gb[i] = tr_read(aB + 64 * (i >> 1) + 8 * (i & 1));
    }
    __builtin_amdgcn_s_setprio(3);
#pragma unroll
    for (int i = 0; i < 6; ++i)
        acc[i] = __builtin_amdgcn_mfma_f32_16x16x32_bf16(
            __builtin_bit_cast(bf8, __builtin_shufflevector(ga[i], gb[i], 0, 1, 2, 3, 4, 5, 6, 7)),
            __builtin_bit_cast(bf8, bw), acc[i], 0, 0, 0);
    __builtin_amdgcn_s_setprio(1);
}

__device__ __forceinline__ u32 slot_addr(u32 par, u32 pos) {
    return POOL + (par ? NS - 1 - pos : pos) * PXB + ((pos & 4) << 3) + ((pos & 8) << 1);
}

__device__ __forceinline__ void run_batches(int b0, int b1, int step, int n, u32 par, u32 lb, const BatchCtx& c, const char* x) {
#pragma unroll 1
    for (int b = b0; b < b1; b += step) {
        u32 idx = min(c.l4 + 16 * b, (u32)(n - 1));
        u32x2 d = *LDSP(u32x2, lb + idx * 8);
        u32 w1 = *LDSP(u32, lb + NS * 8 + idx * 4);
        u32 aA = ((d[0] & 0xffffu) * 16) ^ (2 * c.c8), aB = ((d[0] >> 16) * 16) ^ (2 * c.c8), wp = d[1] & 0x7fff7fffu;
        bool two = (d[1] & 0x8000u) != 0;
        f4 acc[6];
#pragma unroll
        for (int i = 0; i < 6; ++i) acc[i] = f4{0.f, 0.f, 0.f, 0.f};
        interp_pass(acc, aA, aB, wp, c);
        if (__builtin_amdgcn_ballot_w64(two)) {
            u32 dn = two ? c.pitch : 0;
            interp_pass(acc, aA + dn, aB + dn, two ? w1 : 0u, c);
        }
        u32 e0 = slot_addr(par, 16 * b + c.col) ^ (4 * (c.l4 & 12));
#pragma unroll
        for (int k = 0; k < 3; ++k)
            *LDSP(u32x4, e0 + 64 * k) = u32x4{pk_bf16(acc[2 * k][0], acc[2 * k][1]), pk_bf16(acc[2 * k][2], acc[2 * k][3]),
                                              pk_bf16(acc[2 * k + 1][0], acc[2 * k + 1][1]), pk_bf16(acc[2 * k + 1][2], acc[2 * k + 1][3])};
        if ((int)d[1] < 0) {
            float py = lo_f(wp), px = hi_f(wp);
            float wy = __builtin_amdgcn_fractf(py), wx = __builtin_amdgcn_fractf(px);
            u32 ab = pk_bf16(1.f - wy, 1.f - wx);
            u32 w0 = pk_bf16(lo_f(ab) * hi_f(ab), lo_f(ab) * wx);
            w1 = pk_bf16(wy * hi_f(ab), wy * wx);
            u32 g0 = ((u32)py * IMG + (u32)px) * PXB + 2 * c.c8, dx = wx != 0.f ? PXB : 0, dy = wy != 0.f ? ROWB : 0;
            u32 e = slot_addr(par, 16 * b + c.l4) ^ (2 * c.c8);
#pragma unroll
            for (int k = 0; k < 192; k += 64) {
                u32x4 v0 = *(const u32x4*)(x + g0 + k), v1 = *(const u32x4*)(x + g0 + k + dx);
                u32x4 v2 = *(const u32x4*)(x + g0 + k + dy), v3 = *(const u32x4*)(x + g0 + k + dy + dx), o;
#pragma unroll
                for (int i = 0; i < 4; ++i)
                    o[i] = pk_bf16(lo_f(w0) * lo_f(v0[i]) + hi_f(w0) * lo_f(v1[i]) + lo_f(w1) * lo_f(v2[i]) + hi_f(w1) * lo_f(v3[i]),
                                   lo_f(w0) * hi_f(v0[i]) + hi_f(w0) * hi_f(v1[i]) + lo_f(w1) * hi_f(v2[i]) + hi_f(w1) * hi_f(v3[i]));
                *LDSP(u32x4, e + k) = o;
            }
        }
    }
}

__global__ __launch_bounds__(768, 3) void dcn_kernel(const char* x, const char* off,
                                                     const char* wt, char* out) {
    __shared__ __attribute__((aligned(64))) char smem[LDS_BYTES];
    u32x8 ptr;
    int xs, ys, R, wy0, wx0, wrows, wcols, wp;
    u32 lin, wv, xr, bid;
    u64 re, ka;
    asm volatile(
        "s_mov_b64 s[60:61], %[ka]\n\t"
        "s_mov_b32 s62, %[bid]\n\t"
        "v_mov_b32 v17, %[tid]\n\t"
        "s_getpc_b64 %[re]\n\t"
        "v_readfirstlane_b32 %[wv], v17\n\t"
        "s_lshr_b32 s46, %[wv], 8\n\t"
        "s_cmp_eq_u32 s46, 2\n\t"
        "s_cbranch_scc1 10f\n\t"
        "11:\n\t"
        "s_load_dwordx8 %[ptr], s[60:61], 0\n\t"
        "s_lshl_b32 s40, s46, 3\n\t"
        "s_load_dwordx2 s[48:49], s[60:61], s40\n\t"
        "s_cmp_eq_u32 s46, 1\n\t"
        "s_cbranch_scc0 15f\n\t"
        "s_setprio 3\n\t"
        "15:\n\t"
        "s_lshr_b32 %[wv], %[wv], 6\n\t"
        "s_and_b32 %[bo], s62, 0xff\n\t"
        "s_mov_b64 %[ko], s[60:61]\n\t"
        "s_lshl_b32 s40, s62, 2\n\t"
        "s_lshr_b32 s40, 0x84e6a0c2, s40\n\t"
        "s_and_b32 s40, s40, 15\n\t"
        "s_lshr_b32 s41, %[bo], 3\n\t"
        "s_lshl4_add_u32 %[lin], s40, s41\n\t"
        "s_sub_u32 s42, %[lin], 113\n\t"
        "s_cbranch_scc1 24f\n\t"
        "s_mul_hi_u32 s40, s42, 330382100\n\t"
        "s_mul_i32 s41, s40, 13\n\t"
        "s_sub_u32 s42, s42, s41\n\t"
        "s_add_u32 s42, s42, 4\n\t"
        "25:\n\t"
        "s_lshl4_add_u32 %[xs], s40, 48\n\t"
        "s_xor_b32 s40, s62, %[bo]\n\t"
        "s_cselect_b32 s40, 0, s42\n\t"
        "s_cmp_lt_u32 s40, 4\n\t"
        "s_cbranch_scc1 20f\n\t"
        "s_mul_i32 %[wy], s42, 14\n\t"
        "s_sub_u32 %[wy], %[wy], 16\n\t"
        "s_add_u32 %[ys], %[wy], 2\n\t"
        "s_sub_u32 %[wx], %[xs], 2\n\t"
        "s_mov_b32 %[R], 14\n\t"
        "s_min_u32 s40, %[ys], %[xs]\n\t"
        "s_cmp_ge_u32 s40, 67\n\t"
        "s_cselect_b32 %[xr], 2, 1\n\t"
        "s_and_b32 s47, %[wv], 3\n\t"
        "s_mov_b32 s51, 0x20000\n\t"
        "s_cmp_lg_u32 s46, 0\n\t"
        "s_cbranch_scc1 21f\n\t"
        "v_and_b32 v10, 63, v17\n\t"
        "s_mul_i32 s52, s47, 640\n\t"
        "v_cmp_gt_u32 vcc, 40, v10\n\t"
        "s_bitcmp1_b32 s47, 0\n\t"
        "v_lshrrev_b32 v11, 2, v10\n\t"
        "s_cselect_b32 s42, 10, 0\n\t"
        "v_and_b32 v10, 3, v10\n\t"
        "s_add_u32 s42, s42, %[wx]\n\t"
        "v_add_u32 v11, s42, v11\n\t"
        "s_lshr_b32 s43, s47, 1\n\t"
        "v_min_u32 v12, 223, v11\n\t"
        "s_add_u32 s43, s43, %[wy]\n\t"
        "v_mul_u32_u24 v12, 12, v12\n\t"
        "s_mul_i32 s54, s43, 43008\n\t"
        "v_add_lshl_u32 v12, v12, v10, 4\n\t"
        "s_movk_i32 s53, 2560\n\t"
        "v_lshlrev_b32 v11, 3, v11\n\t"
        "s_mov_b32 s55, 86016\n\t"
        "v_bitop3_b32 v10, v12, v11, 32 bitop3:0x78\n\t"
        "s_mov_b32 s56, 0\n\t"
        "s_mov_b32 s57, 9\n\t"
        "s_mov_b32 s58, 9590784\n\t"
        "s_mov_b32 s50, 9633792\n\t"
        "s_mov_b32 s46, 2\n\t"
        "s_barrier\n\t"
        "s_branch 7f\n\t"
        "21:\n\t"
        "s_mov_b32 s57, 0\n\t"
        "s_cmp_eq_u32 s46, 2\n\t"
        "s_cbranch_scc1 5f\n\t"
        "s_cmp_lt_u32 %[xr], 2\n\t"
        "s_cbranch_scc1 7f\n\t"
        "s_sleep 20\n\t"
        "s_branch 7f\n\t"
        "20:\n\t"
        "s_mov_b32 s44, 470812672\n\t"
        "s_mov_b32 s45, 1413888042\n\t"
        "s_mov_b32 s43, 14\n\t"
        "3:\n\t"
        "s_min_u32 s40, s42, 7\n\t"
        "s_sub_u32 s41, s42, s40\n\t"
        "s_lshl_b32 s40, s40, 3\n\t"
        "s_lshr_b64 s[44:45], s[44:45], s40\n\t"
        "s_and_b32 %[ys], s44, 0xff\n\t"
        "s_bfe_u32 s45, s44, 0x80008\n\t"
        "s_sub_u32 s45, s45, %[ys]\n\t"
        "s_cmp_lt_u32 s42, 7\n\t"
        "s_cselect_b32 %[R], s45, s43\n\t"
        "s_mul_i32 s41, s41, s43\n\t"
        "s_add_u32 %[ys], %[ys], s41\n\t"
        "s_sub_u32 s40, 224, %[ys]\n\t"
        "s_lshl_b32 s41, s43, 1\n\t"
        "s_cmp_lt_u32 s40, s41\n\t"
        "s_cselect_b32 %[R], s40, %[R]\n\t"
        "s_sub_u32 s40, %[xs], 16\n\t"
        "s_cmp_le_u32 s40, 16\n\t"
        "s_cselect_b32 %[wc], 21, 20\n\t"
        "s_subb_u32 %[wx], %[xs], 2\n\t"
        "s_max_i32 %[wx], %[wx], 0\n\t"
        "s_cmp_le_u32 %[R], 8\n\t"
        "s_cselect_b32 %[wp], 24, %[wc]\n\t"
        "s_cmp_lt_u32 %[ys], 36\n\t"
        "s_subb_u32 %[wy], %[ys], 2\n\t"
        "s_max_i32 %[wy], %[wy], 0\n\t"
        "s_add_u32 s40, %[ys], %[R]\n\t"
        "s_cmp_lt_u32 s40, 32\n\t"
        "s_addc_u32 s40, s40, 2\n\t"
        "s_sub_u32 %[wr], s40, %[wy]\n\t"
        "s_mov_b32 %[xr], 0\n\t"
        "s_and_b32 s47, %[wv], 3\n\t"
        "s_mov_b32 s51, 0x20000\n\t"
        "s_cmp_eq_u32 s46, 1\n\t"
        "s_cbranch_scc0 5f\n\t"
        "s_mul_i32 s52, s47, 6400\n\t"
        "s_add_u32 s52, s52, 110592\n\t"
        "s_movk_i32 s53, 800\n\t"
        "s_lshl2_add_u32 s40, s47, %[ys]\n\t"
        "s_mul_i32 s40, s40, 224\n\t"
        "s_add_u32 s40, s40, %[xs]\n\t"
        "s_mul_i32 s54, s40, 100\n\t"
        "s_movk_i32 s55, 800\n\t"
        "s_movk_i32 s56, 22336\n\t"
        "s_lshl_b32 s47, s47, 2\n\t"
        "s_sub_u32 s57, %[R], s47\n\t"
        "s_min_i32 s57, s57, 4\n\t"
        "s_lshl_b32 s57, s57, 1\n\t"
        "s_mov_b32 s50, 5017600\n\t"
        "s_mov_b32 s58, -1\n\t"
        "v_and_b32 v10, 63, v17\n\t"
        "v_cmp_gt_u32 vcc, 50, v10\n\t"
        "v_lshlrev_b32 v10, 4, v10\n\t"
        "7:\n\t"
        "s_waitcnt lgkmcnt(0)\n\t"
        "s_and_b32 s49, s49, 0xffff\n\t"
        "s_cmp_lt_i32 s57, 1\n\t"
        "s_cbranch_scc1 9f\n\t"
        "s_mov_b64 exec, vcc\n\t"
        "8:\n\t"
        "s_mov_b32 m0, s52\n\t"
        "s_add_u32 s52, s52, s53\n\t"
        "buffer_load_dwordx4 v10, s[48:51], s54 offen lds\n\t"
        "s_add_u32 s54, s54, s55\n\t"
        "s_xor_b32 s55, s55, s56\n\t"
        "s_min_u32 s54, s54, s58\n\t"
        "s_sub_u32 s57, s57, 1\n\t"
        "s_cmp_lg_u32 s57, 0\n\t"
        "s_cbranch_scc1 8b\n\t"
        "s_mov_b64 exec, -1\n\t"
        "9:\n\t"
        "s_cmp_eq_u32 s46, 2\n\t"
        "s_cbranch_scc1 13f\n\t"
        "s_cmp_eq_u32 s46, 0\n\t"
        "s_cbranch_scc1 22f\n\t"
        "s_barrier\n\t"
        "s_branch 13f\n\t"
        "22:\n\t"
        "s_barrier\n\t"
        "s_mov_b32 s46, 2\n\t"
        "s_mov_b32 s57, 3\n\t"
        "s_mov_b64 exec, vcc\n\t"
        "s_branch 8b\n\t"
        "24:\n\t"
        "s_sub_u32 s40, %[lin], 102\n\t"
        "s_mov_b32 s42, 2\n\t"
        "s_cbranch_scc0 25b\n\t"
        "s_sub_u32 s41, %[lin], 33\n\t"
        "s_cbranch_scc0 2f\n\t"
        "s_mul_i32 s41, %[lin], 373\n\t"
        "s_lshr_b32 s41, s41, 12\n\t"
        "s_mul_i32 s40, s41, 11\n\t"
        "s_sub_u32 s40, %[lin], s40\n\t"
        "s_lshr_b32 s42, 3, s41\n\t"
        "s_branch 25b\n\t"
        "2:\n\t"
        "s_mov_b32 s44, 470812672\n\t"
        "s_mov_b32 s45, 1363359273\n\t"
        "s_mov_b32 s43, 13\n\t"
        "s_mov_b32 %[xs], 32\n\t"
        "s_sub_u32 s42, s41, 51\n\t"
        "s_cbranch_scc0 3b\n\t"
        "s_mov_b32 s44, 269092096\n\t"
        "s_mov_b32 s45, 942415896\n\t"
        "s_sub_u32 s42, s41, 30\n\t"
        "s_cselect_b32 s42, s41, s42\n\t"
        "s_cselect_b32 %[xs], 0, 16\n\t"
        "s_cselect_b32 s43, 8, 12\n\t"
        "s_cselect_b32 s44, 218629120, s44\n\t"
        "s_cselect_b32 s45, 673192211, s45\n\t"
        "s_branch 3b\n\t"
        "10:\n\t"
        "s_setprio 2\n\t"
        "v_and_b32 v11, 0xff, v17\n\t"
        "v_mov_b32 v10, 153600\n\t"
        "v_mov_b32 v12, 0\n\t"
        "v_mov_b32 v13, 0\n\t"
        "v_mov_b32 v14, 0\n\t"
        "v_mov_b32 v15, 0\n\t"
        "v_cmp_eq_u32 vcc, 0, v11\n\t"
        "v_lshl_add_u32 v10, v11, 4, v10\n\t"
        "v_mov_b32 v16, 208\n\t"
        "ds_write_b128 v10, v[12:15] offset:4096\n\t"
        "v_cndmask_b32 v12, 0, v16, vcc\n\t"
        "ds_write_b128 v10, v[12:15]\n\t"
        "s_cmp_gt_u32 %[wv], 0x23f\n\t"
        "s_cbranch_scc1 14f\n\t"
        "s_bfe_u32 s42, s62, 0x50003\n\t"
        "s_mul_i32 s42, s42, 10\n\t"
        "s_getpc_b64 s[40:41]\n\t"
        "s_mov_b32 m0, 73152\n\t"
        "v_add_u32 v11, s42, v11\n\t"
        "v_min_u32 v11, 294, v11\n\t"
        "v_lshlrev_b32 v11, 7, v11\n\t"
        "s_mov_b64 exec, 1023\n\t"
        "global_load_lds_dword v11, s[40:41]\n\t"
        "s_mov_b64 exec, -1\n\t"
        "14:\n\t"
        "s_cmp_gt_u32 %[wv], 0x2bf\n\t"
        "s_cbranch_scc1 19f\n\t"
        "s_waitcnt lgkmcnt(0)\n\t"
        "s_barrier\n\t"
        "s_branch 11b\n\t"
        "5:\n\t"
        "v_and_b32 v10, 0xff, v17\n\t"
        "v_mul_u32_u24 v11, 171, v10\n\t"
        "v_lshlrev_b32 v12, 4, v10\n\t"
        "v_lshrrev_b32 v11, 11, v11\n\t"
        "s_lshl_b32 s52, s47, 10\n\t"
        "s_cmp_eq_u32 s46, 2\n\t"
        "s_cbranch_scc1 6f\n\t"
        "s_mul_i32 s53, %[wp], 192\n\t"
        "s_mul_i32 s40, %[wx], 192\n\t"
        "s_mul_i32 s54, %[wy], 43008\n\t"
        "s_add_u32 s54, s54, s40\n\t"
        "s_add_u32 s58, s40, 9590784\n\t"
        "s_mov_b32 s55, 43008\n\t"
        "s_mov_b32 s56, 0\n\t"
        "s_sub_u32 s57, %[wr], 3\n\t"
        "s_mov_b32 s50, 9633792\n\t"
        "s_movk_i32 s40, -192\n\t"
        "s_movk_i32 s41, 223\n\t"
        "v_cmp_gt_u32 vcc, %[wc], v11\n\t"
        "v_add_u32 v11, %[wx], v11\n\t"
        "v_lshlrev_b32 v10, 3, v11\n\t"
        "v_sub_u32 v11, v11, s41 clamp\n\t"
        "v_bitop3_b32 v10, v12, v10, 32 bitop3:0x78\n\t"
        "v_mad_i32_i24 v10, v11, s40, v10\n\t"
        "s_barrier\n\t"
        "s_branch 7b\n\t"
        "6:\n\t"
        "s_cmp_le_u32 %[R], 8\n\t"
        "s_cselect_b32 s40, 4, 2\n\t"
        "s_cselect_b32 s57, 4, 8\n\t"
        "s_cmp_lg_u32 %[xr], 0\n\t"
        "s_cselect_b32 s40, 0, s40\n\t"
        "s_cselect_b32 s57, 10, s57\n\t"
        "s_cselect_b32 s56, 86016, 0\n\t"
        "s_mulk_i32 s40, 6144\n\t"
        "s_add_u32 s52, s52, s40\n\t"
        "s_add_u32 s52, s52, 73728\n\t"
        "s_cmp_eq_u32 s47, 3\n\t"
        "s_movk_i32 s53, 3072\n\t"
        "s_mov_b32 s54, 0\n\t"
        "s_movk_i32 s55, 3072\n\t"
        "s_mov_b32 s50, 460800\n\t"
        "s_mov_b32 s58, -1\n\t"
        "s_cselect_b32 s57, 0, s57\n\t"
        "s_mov_b64 vcc, -1\n\t"
        "v_lshlrev_b32 v10, 2, v11\n\t"
        "v_bitop3_b32 v10, v12, v10, 32 bitop3:0x78\n\t"
        "s_branch 7b\n\t"
        "19:\n\t"
        "s_load_dwordx2 s[48:49], s[60:61], 16\n\t"
        "s_mov_b32 s50, 460800\n\t"
        "s_mov_b32 s51, 0x20000\n\t"
        "s_bfe_u32 s40, s62, 0x50003\n\t"
        "s_mul_i32 s40, s40, 33\n\t"
        "v_and_b32 v10, 63, v17\n\t"
        "v_cmp_gt_u32 vcc, 33, v10\n\t"
        "v_add_u32 v10, s40, v10\n\t"
        "v_mul_u32_u24 v11, 1366, v10\n\t"
        "v_lshrrev_b32 v11, 16, v11\n\t"
        "v_sub_u32 v12, v11, 7 clamp\n\t"
        "v_mul_u32_u24 v13, 43, v12\n\t"
        "v_lshrrev_b32 v13, 7, v13\n\t"
        "v_mul_u32_u24 v13, 6, v13\n\t"
        "v_lshl_add_u32 v13, v12, 1, v13\n\t"
        "v_sub_u32 v13, v13, 7 clamp\n\t"
        "v_mad_u32_u24 v10, v13, 48, v10\n\t"
        "v_lshlrev_b32 v10, 7, v10\n\t"
        "s_mov_b32 m0, 73152\n\t"
        "s_mov_b64 exec, vcc\n\t"
        "s_waitcnt lgkmcnt(0)\n\t"
        "s_and_b32 s49, s49, 0xffff\n\t"
        "buffer_load_dword v10, s[48:51], 0 offen lds\n\t"
        "s_mov_b64 exec, -1\n\t"
        "s_barrier\n\t"
        "s_branch 11b\n\t"
        "13:\n\t"
        : [ptr] "=&s"(ptr), [xs] "=&s"(xs), [ys] "=&s"(ys), [R] "=&s"(R), [wy] "=&s"(wy0), [wx] "=&s"(wx0), [wr] "=&s"(wrows),
          [wc] "=&s"(wcols), [wp] "=&s"(wp), [lin] "=&s"(lin), [wv] "=&s"(wv), [xr] "=&s"(xr), [re] "=&s"(re), [ko] "=&s"(ka), [bo] "=&s"(bid)
        : [ka] "s"(__builtin_amdgcn_kernarg_segment_ptr()), [bid] "s"(blockIdx.x), [tid] "v"(threadIdx.x)
        : "memory", "vcc", "scc", "s40", "s41", "s42", "s43", "s44", "s45", "s46", "s47", "s48", "s49", "s50", "s51", "s52", "s53", "s54",
          "s55", "s56", "s57", "s58", "s60", "s61", "s62", "v10", "v11", "v12", "v13", "v14", "v15", "v16", "v17");
    const u32 pitch = wp * PXB;
    __builtin_amdgcn_sched_barrier(0);
    asm volatile("" :: "s"((u32)(size_t)(LDS(char)*)smem + wv));
    const bool ic = R <= 8;
    const u32x4 rw = make_rsrc(ptr[4], ptr[5], 25 * STAGE);

    if (wv < 4) {
        const u32x4 ro = make_rsrc(ptr[6], ptr[7], IMG * ROWB);
        u32 tn = 12;
        if (!ic) {
            LANE_VARS;
            u32 ca = CNT;
            const u32 mg = wv >> 1, nh = wv & 1, wb = WB;
            const u32 wa0 = wb + 128 * nh, ob = ((ys + mg * ((R + 1) >> 1)) * IMG + xs + p) * PXB + ((q & 2) << 3);
            asm volatile(
        "s_cmp_eq_u32 %[xr], 0\n\t"
        "s_cbranch_scc1 1f\n\t"
        "s_mov_b32 s40, 0\n\t"
        "s_mov_b32 s41, 0\n\t"
        "v_add_u32 v12, s40, %[px]\n\t"
        "s_add_u32 s40, s40, 1\n\t"
        "v_lshlrev_b32 v12, 3, v12\n\t"
        "s_cmp_eq_u32 s40, 5\n\t"
        "v_bitop3_b32 v12, %[q16], v12, 32 bitop3:0x78\n\t"
        "s_cselect_b32 s42, 22784, 64\n\t"
        "v_add3_u32 v14, v12, %[rb], s41\n\t"
        "s_cselect_b32 s40, 0, s40\n\t"
        "s_add_u32 s41, s41, s42\n\t"
        "v_mov_b64 v[84:85], 0\n\t"
        "v_mov_b64 v[86:87], 0\n\t"
        "v_mov_b64 v[88:89], 0\n\t"
        "v_mov_b64 v[90:91], 0\n\t"
        "v_mov_b64 v[92:93], 0\n\t"
        "v_mov_b64 v[94:95], 0\n\t"
        "v_mov_b64 v[96:97], 0\n\t"
        "v_mov_b64 v[98:99], 0\n\t"
        "v_mov_b64 v[100:101], 0\n\t"
        "v_mov_b64 v[102:103], 0\n\t"
        "v_mov_b64 v[104:105], 0\n\t"
        "v_mov_b64 v[106:107], 0\n\t"
        "v_mov_b64 v[108:109], 0\n\t"
        "v_mov_b64 v[110:111], 0\n\t"
        "v_mov_b64 v[112:113], 0\n\t"
        "v_mov_b64 v[114:115], 0\n\t"
        "v_mov_b64 v[116:117], 0\n\t"
        "v_mov_b64 v[118:119], 0\n\t"
        "v_mov_b64 v[120:121], 0\n\t"
        "v_mov_b64 v[122:123], 0\n\t"
        "v_mov_b64 v[124:125], 0\n\t"
        "v_mov_b64 v[126:127], 0\n\t"
        "v_mov_b64 v[128:129], 0\n\t"
        "v_mov_b64 v[130:131], 0\n\t"
        "v_mov_b64 v[132:133], 0\n\t"
        "v_mov_b64 v[134:135], 0\n\t"
        "v_mov_b64 v[136:137], 0\n\t"
        "v_mov_b64 v[138:139], 0\n\t"
        "v_mov_b64 v[140:141], 0\n\t"
        "v_mov_b64 v[142:143], 0\n\t"
        "v_mov_b64 v[144:145], 0\n\t"
        "v_mov_b64 v[146:147], 0\n\t"
        "v_mov_b64 v[148:149], 0\n\t"
        "v_mov_b64 v[150:151], 0\n\t"
        "v_mov_b64 v[152:153], 0\n\t"
        "v_mov_b64 v[154:155], 0\n\t"
        "v_mov_b64 v[156:157], 0\n\t"
        "v_mov_b64 v[158:159], 0\n\t"
        "v_mov_b64 v[160:161], 0\n\t"
        "v_mov_b64 v[162:163], 0\n\t"
        "v_mov_b64 v[164:165], 0\n\t"
        "v_mov_b64 v[166:167], 0\n\t"
        "s_waitcnt vmcnt(0)\n\t"
        "s_barrier\n\t"
        "ds_read_b32 v13, %[ca] offset:112\n\t"
        "ds_read_b64_tr_b16 v[16:17], %[wa0] offset:0\n\t"
        "ds_read_b64_tr_b16 v[18:19], %[wa0] offset:768\n\t"
        "ds_read_b64_tr_b16 v[20:21], %[wa1] offset:0\n\t"
        "ds_read_b64_tr_b16 v[22:23], %[wa1] offset:768\n\t"
        "ds_read_b64_tr_b16 v[24:25], %[wa2] offset:0\n\t"
        "ds_read_b64_tr_b16 v[26:27], %[wa2] offset:768\n\t"
        "ds_read_b128 v[40:43], v14 offset:0\n\t"
        "ds_read_b128 v[44:47], v14 offset:1280\n\t"
        "ds_read_b128 v[48:51], v14 offset:2560\n\t"
        "ds_read_b128 v[52:55], v14 offset:3840\n\t"
        "ds_read_b128 v[56:59], v14 offset:5120\n\t"
        "ds_read_b128 v[60:63], v14 offset:6400\n\t"
        "ds_read_b128 v[64:67], v14 offset:7680\n\t"
        "s_waitcnt lgkmcnt(13)\n\t"
        "v_cmp_eq_u32 vcc, 1, v13\n\t"
        "s_mov_b32 %[tn], 6\n\t"
        "s_waitcnt lgkmcnt(0)\n\t"
        "s_cbranch_vccnz 2f\n\t"
        ".p2align 6\n\t"
        "80:\n\t"
        "v_mfma_f32_16x16x32_bf16 v[84:87], v[16:19], v[40:43], v[84:87]\n\t"
        "ds_read_b64_tr_b16 v[28:29], %[wa0] offset:6144\n\t"
        "v_mfma_f32_16x16x32_bf16 v[88:91], v[20:23], v[40:43], v[88:91]\n\t"
        "ds_read_b64_tr_b16 v[30:31], %[wa0] offset:6912\n\t"
        "v_add_u32 v12, s40, %[px]\n\t"
        "v_mfma_f32_16x16x32_bf16 v[92:95], v[24:27], v[40:43], v[92:95]\n\t"
        "ds_read_b64_tr_b16 v[32:33], %[wa1] offset:6144\n\t"
        "s_add_u32 s40, s40, 1\n\t"
        "v_mfma_f32_16x16x32_bf16 v[104:107], v[24:27], v[44:47], v[104:107]\n\t"
        "ds_read_b64_tr_b16 v[34:35], %[wa1] offset:6912\n\t"
        "v_lshlrev_b32 v12, 3, v12\n\t"
        "v_mfma_f32_16x16x32_bf16 v[100:103], v[20:23], v[44:47], v[100:103]\n\t"
        "ds_read_b64_tr_b16 v[36:37], %[wa2] offset:6144\n\t"
        "s_cmp_eq_u32 s40, 5\n\t"
        "v_mfma_f32_16x16x32_bf16 v[96:99], v[16:19], v[44:47], v[96:99]\n\t"
        "ds_read_b64_tr_b16 v[38:39], %[wa2] offset:6912\n\t"
        "v_bitop3_b32 v12, %[q16], v12, 32 bitop3:0x78\n\t"
        "v_mfma_f32_16x16x32_bf16 v[108:111], v[16:19], v[48:51], v[108:111]\n\t"
        "s_cselect_b32 s42, 22784, 64\n\t"
        "v_mfma_f32_16x16x32_bf16 v[112:115], v[20:23], v[48:51], v[112:115]\n\t"
        "v_add3_u32 v15, v12, %[rb], s41\n\t"
        "v_mfma_f32_16x16x32_bf16 v[116:119], v[24:27], v[48:51], v[116:119]\n\t"
        "s_cselect_b32 s40, 0, s40\n\t"
        "v_mfma_f32_16x16x32_bf16 v[128:131], v[24:27], v[52:55], v[128:131]\n\t"
        "s_add_u32 s41, s41, s42\n\t"
        "v_mfma_f32_16x16x32_bf16 v[124:127], v[20:23], v[52:55], v[124:127]\n\t"
        "ds_read_b128 v[68:71], v14 offset:8960\n\t"
        "v_mfma_f32_16x16x32_bf16 v[120:123], v[16:19], v[52:55], v[120:123]\n\t"
        "v_mfma_f32_16x16x32_bf16 v[132:135], v[16:19], v[56:59], v[132:135]\n\t"
        "ds_read_b128 v[40:43], v15 offset:0\n\t"
        "v_mfma_f32_16x16x32_bf16 v[136:139], v[20:23], v[56:59], v[136:139]\n\t"
        "v_mfma_f32_16x16x32_bf16 v[140:143], v[24:27], v[56:59], v[140:143]\n\t"
        "v_mfma_f32_16x16x32_bf16 v[152:155], v[24:27], v[60:63], v[152:155]\n\t"
        "v_mfma_f32_16x16x32_bf16 v[148:151], v[20:23], v[60:63], v[148:151]\n\t"
        "v_mfma_f32_16x16x32_bf16 v[144:147], v[16:19], v[60:63], v[144:147]\n\t"
        "v_mfma_f32_16x16x32_bf16 v[156:159], v[16:19], v[64:67], v[156:159]\n\t"
        "v_mfma_f32_16x16x32_bf16 v[160:163], v[20:23], v[64:67], v[160:163]\n\t"
        "s_waitcnt lgkmcnt(2)\n\t"
        "v_mfma_f32_16x16x32_bf16 v[164:167], v[24:27], v[64:67], v[164:167]\n\t"
        "v_mfma_f32_16x16x32_bf16 v[84:87], v[28:31], v[44:47], v[84:87]\n\t"
        "ds_read_b64_tr_b16 v[16:17], %[wa0] offset:12288\n\t"
        "v_mfma_f32_16x16x32_bf16 v[88:91], v[32:35], v[44:47], v[88:91]\n\t"
        "ds_read_b64_tr_b16 v[18:19], %[wa0] offset:13056\n\t"
        "v_mfma_f32_16x16x32_bf16 v[92:95], v[36:39], v[44:47], v[92:95]\n\t"
        "ds_read_b128 v[44:47], v15 offset:1280\n\t"
        "v_mfma_f32_16x16x32_bf16 v[104:107], v[36:39], v[48:51], v[104:107]\n\t"
        "ds_read_b64_tr_b16 v[20:21], %[wa1] offset:12288\n\t"
        "v_mfma_f32_16x16x32_bf16 v[100:103], v[32:35], v[48:51], v[100:103]\n\t"
        "ds_read_b64_tr_b16 v[22:23], %[wa1] offset:13056\n\t"
        "v_mfma_f32_16x16x32_bf16 v[96:99], v[28:31], v[48:51], v[96:99]\n\t"
        "ds_read_b64_tr_b16 v[24:25], %[wa2] offset:12288\n\t"
        "v_mfma_f32_16x16x32_bf16 v[108:111], v[28:31], v[52:55], v[108:111]\n\t"
        "ds_read_b64_tr_b16 v[26:27], %[wa2] offset:13056\n\t"
        "v_mfma_f32_16x16x32_bf16 v[112:115], v[32:35], v[52:55], v[112:115]\n\t"
        "ds_read_b128 v[72:75], v14 offset:10240\n\t"
        "v_mfma_f32_16x16x32_bf16 v[116:119], v[36:39], v[52:55], v[116:119]\n\t"
        "v_mfma_f32_16x16x32_bf16 v[128:131], v[36:39], v[56:59], v[128:131]\n\t"
        "v_mfma_f32_16x16x32_bf16 v[124:127], v[32:35], v[56:59], v[124:127]\n\t"
        "v_mfma_f32_16x16x32_bf16 v[120:123], v[28:31], v[56:59], v[120:123]\n\t"
        "v_mfma_f32_16x16x32_bf16 v[132:135], v[28:31], v[60:63], v[132:135]\n\t"
        "v_mfma_f32_16x16x32_bf16 v[136:139], v[32:35], v[60:63], v[136:139]\n\t"
        "v_mfma_f32_16x16x32_bf16 v[140:143], v[36:39], v[60:63], v[140:143]\n\t"
        "v_mfma_f32_16x16x32_bf16 v[152:155], v[36:39], v[64:67], v[152:155]\n\t"
        "v_mfma_f32_16x16x32_bf16 v[148:151], v[32:35], v[64:67], v[148:151]\n\t"
        "v_mfma_f32_16x16x32_bf16 v[144:147], v[28:31], v[64:67], v[144:147]\n\t"
        "s_waitcnt lgkmcnt(9)\n\t"
        "v_mfma_f32_16x16x32_bf16 v[156:159], v[28:31], v[68:71], v[156:159]\n\t"
        "v_mfma_f32_16x16x32_bf16 v[160:163], v[32:35], v[68:71], v[160:163]\n\t"
        "s_waitcnt lgkmcnt(1)\n\t"
        "v_mfma_f32_16x16x32_bf16 v[164:167], v[36:39], v[68:71], v[164:167]\n\t"
        "v_mfma_f32_16x16x32_bf16 v[84:87], v[16:19], v[48:51], v[84:87]\n\t"
        "ds_read_b64_tr_b16 v[28:29], %[wa0] offset:18432\n\t"
        "v_mfma_f32_16x16x32_bf16 v[88:91], v[20:23], v[48:51], v[88:91]\n\t"
        "ds_read_b64_tr_b16 v[30:31], %[wa0] offset:19200\n\t"
        "v_mfma_f32_16x16x32_bf16 v[92:95], v[24:27], v[48:51], v[92:95]\n\t"
        "ds_read_b128 v[48:51], v15 offset:2560\n\t"
        "v_mfma_f32_16x16x32_bf16 v[104:107], v[24:27], v[52:55], v[104:107]\n\t"
        "ds_read_b64_tr_b16 v[32:33], %[wa1] offset:18432\n\t"
        "v_mfma_f32_16x16x32_bf16 v[100:103], v[20:23], v[52:55], v[100:103]\n\t"
        "ds_read_b64_tr_b16 v[34:35], %[wa1] offset:19200\n\t"
        "v_mfma_f32_16x16x32_bf16 v[96:99], v[16:19], v[52:55], v[96:99]\n\t"
        "ds_read_b64_tr_b16 v[36:37], %[wa2] offset:18432\n\t"
        "v_mfma_f32_16x16x32_bf16 v[108:111], v[16:19], v[56:59], v[108:111]\n\t"
        "ds_read_b64_tr_b16 v[38:39], %[wa2] offset:19200\n\t"
        "v_mfma_f32_16x16x32_bf16 v[112:115], v[20:23], v[56:59], v[112:115]\n\t"
        "ds_read_b128 v[76:79], v14 offset:11520\n\t"
        "v_mfma_f32_16x16x32_bf16 v[116:119], v[24:27], v[56:59], v[116:119]\n\t"
        "v_mfma_f32_16x16x32_bf16 v[128:131], v[24:27], v[60:63], v[128:131]\n\t"
        "v_mfma_f32_16x16x32_bf16 v[124:127], v[20:23], v[60:63], v[124:127]\n\t"
        "v_mfma_f32_16x16x32_bf16 v[120:123], v[16:19], v[60:63], v[120:123]\n\t"
        "v_mfma_f32_16x16x32_bf16 v[132:135], v[16:19], v[64:67], v[132:135]\n\t"
        "v_mfma_f32_16x16x32_bf16 v[136:139], v[20:23], v[64:67], v[136:139]\n\t"
        "v_mfma_f32_16x16x32_bf16 v[140:143], v[24:27], v[64:67], v[140:143]\n\t"
        "v_mfma_f32_16x16x32_bf16 v[152:155], v[24:27], v[68:71], v[152:155]\n\t"
        "v_mfma_f32_16x16x32_bf16 v[148:151], v[20:23], v[68:71], v[148:151]\n\t"
        "v_mfma_f32_16x16x32_bf16 v[144:147], v[16:19], v[68:71], v[144:147]\n\t"
        "s_waitcnt lgkmcnt(8)\n\t"
        "v_mfma_f32_16x16x32_bf16 v[156:159], v[16:19], v[72:75], v[156:159]\n\t"
        "v_mfma_f32_16x16x32_bf16 v[160:163], v[20:23], v[72:75], v[160:163]\n\t"
        "s_waitcnt lgkmcnt(1)\n\t"
        "v_mfma_f32_16x16x32_bf16 v[164:167], v[24:27], v[72:75], v[164:167]\n\t"
        "v_mfma_f32_16x16x32_bf16 v[84:87], v[28:31], v[52:55], v[84:87]\n\t"
        "ds_read_b64_tr_b16 v[16:17], %[wa0] offset:24576\n\t"
        "v_mfma_f32_16x16x32_bf16 v[88:91], v[32:35], v[52:55], v[88:91]\n\t"
        "ds_read_b64_tr_b16 v[18:19], %[wa0] offset:25344\n\t"
        "v_mfma_f32_16x16x32_bf16 v[92:95], v[36:39], v[52:55], v[92:95]\n\t"
        "ds_read_b128 v[52:55], v15 offset:3840\n\t"
        "v_mfma_f32_16x16x32_bf16 v[104:107], v[36:39], v[56:59], v[104:107]\n\t"
        "ds_read_b64_tr_b16 v[20:21], %[wa1] offset:24576\n\t"
        "v_mfma_f32_16x16x32_bf16 v[100:103], v[32:35], v[56:59], v[100:103]\n\t"
        "ds_read_b64_tr_b16 v[22:23], %[wa1] offset:25344\n\t"
        "v_mfma_f32_16x16x32_bf16 v[96:99], v[28:31], v[56:59], v[96:99]\n\t"
        "ds_read_b64_tr_b16 v[24:25], %[wa2] offset:24576\n\t"
        "v_mfma_f32_16x16x32_bf16 v[108:111], v[28:31], v[60:63], v[108:111]\n\t"
        "ds_read_b64_tr_b16 v[26:27], %[wa2] offset:25344\n\t"
        "v_mfma_f32_16x16x32_bf16 v[112:115], v[32:35], v[60:63], v[112:115]\n\t"
        "ds_read_b128 v[80:83], v14 offset:12800\n\t"
        "v_mfma_f32_16x16x32_bf16 v[116:119], v[36:39], v[60:63], v[116:119]\n\t"
        "v_mfma_f32_16x16x32_bf16 v[128:131], v[36:39], v[64:67], v[128:131]\n\t"
        "v_mfma_f32_16x16x32_bf16 v[124:127], v[32:35], v[64:67], v[124:127]\n\t"
        "v_mfma_f32_16x16x32_bf16 v[120:123], v[28:31], v[64:67], v[120:123]\n\t"
        "v_mfma_f32_16x16x32_bf16 v[132:135], v[28:31], v[68:71], v[132:135]\n\t"
        "v_mfma_f32_16x16x32_bf16 v[136:139], v[32:35], v[68:71], v[136:139]\n\t"
        "v_mfma_f32_16x16x32_bf16 v[140:143], v[36:39], v[68:71], v[140:143]\n\t"
        "v_mfma_f32_16x16x32_bf16 v[152:155], v[36:39], v[72:75], v[152:155]\n\t"
        "v_mfma_f32_16x16x32_bf16 v[148:151], v[32:35], v[72:75], v[148:151]\n\t"
        "v_mfma_f32_16x16x32_bf16 v[144:147], v[28:31], v[72:75], v[144:147]\n\t"
        "s_waitcnt lgkmcnt(8)\n\t"
        "v_mfma_f32_16x16x32_bf16 v[156:159], v[28:31], v[76:79], v[156:159]\n\t"
        "v_mfma_f32_16x16x32_bf16 v[160:163], v[32:35], v[76:79], v[160:163]\n\t"
        "v_mfma_f32_16x16x32_bf16 v[164:167], v[36:39], v[76:79], v[164:167]\n\t"
        "s_waitcnt lgkmcnt(1)\n\t"
        "s_barrier\n\t"
        "v_mfma_f32_16x16x32_bf16 v[84:87], v[16:19], v[56:59], v[84:87]\n\t"
        "ds_read_b64_tr_b16 v[28:29], %[wa0] offset:30720\n\t"
        "v_mfma_f32_16x16x32_bf16 v[88:91], v[20:23], v[56:59], v[88:91]\n\t"
        "ds_read_b64_tr_b16 v[30:31], %[wa0] offset:31488\n\t"
        "v_mfma_f32_16x16x32_bf16 v[92:95], v[24:27], v[56:59], v[92:95]\n\t"
        "ds_read_b128 v[56:59], v15 offset:5120\n\t"
        "v_mfma_f32_16x16x32_bf16 v[104:107], v[24:27], v[60:63], v[104:107]\n\t"
        "ds_read_b64_tr_b16 v[32:33], %[wa1] offset:30720\n\t"
        "v_mfma_f32_16x16x32_bf16 v[100:103], v[20:23], v[60:63], v[100:103]\n\t"
        "ds_read_b64_tr_b16 v[34:35], %[wa1] offset:31488\n\t"
        "v_mfma_f32_16x16x32_bf16 v[96:99], v[16:19], v[60:63], v[96:99]\n\t"
        "ds_read_b128 v[60:63], v15 offset:6400\n\t"
        "v_mfma_f32_16x16x32_bf16 v[108:111], v[16:19], v[64:67], v[108:111]\n\t"
        "ds_read_b64_tr_b16 v[36:37], %[wa2] offset:30720\n\t"
        "v_mfma_f32_16x16x32_bf16 v[112:115], v[20:23], v[64:67], v[112:115]\n\t"
        "ds_read_b64_tr_b16 v[38:39], %[wa2] offset:31488\n\t"
        "v_mfma_f32_16x16x32_bf16 v[116:119], v[24:27], v[64:67], v[116:119]\n\t"
        "ds_read_b128 v[64:67], v15 offset:7680\n\t"
        "v_mfma_f32_16x16x32_bf16 v[128:131], v[24:27], v[68:71], v[128:131]\n\t"
        "v_mfma_f32_16x16x32_bf16 v[124:127], v[20:23], v[68:71], v[124:127]\n\t"
        "v_mfma_f32_16x16x32_bf16 v[120:123], v[16:19], v[68:71], v[120:123]\n\t"
        "v_mfma_f32_16x16x32_bf16 v[132:135], v[16:19], v[72:75], v[132:135]\n\t"
        "v_mfma_f32_16x16x32_bf16 v[136:139], v[20:23], v[72:75], v[136:139]\n\t"
        "v_mfma_f32_16x16x32_bf16 v[140:143], v[24:27], v[72:75], v[140:143]\n\t"
        "v_mfma_f32_16x16x32_bf16 v[152:155], v[24:27], v[76:79], v[152:155]\n\t"
        "v_mfma_f32_16x16x32_bf16 v[148:151], v[20:23], v[76:79], v[148:151]\n\t"
        "v_mfma_f32_16x16x32_bf16 v[144:147], v[16:19], v[76:79], v[144:147]\n\t"
        "s_waitcnt lgkmcnt(9)\n\t"
        "v_mfma_f32_16x16x32_bf16 v[156:159], v[16:19], v[80:83], v[156:159]\n\t"
        "s_sub_u32 %[tn], %[tn], 1\n\t"
        "v_mfma_f32_16x16x32_bf16 v[160:163], v[20:23], v[80:83], v[160:163]\n\t"
        "s_waitcnt lgkmcnt(1)\n\t"
        "v_mfma_f32_16x16x32_bf16 v[164:167], v[24:27], v[80:83], v[164:167]\n\t"
        "s_cbranch_scc1 81f\n\t"
        "v_mfma_f32_16x16x32_bf16 v[84:87], v[28:31], v[40:43], v[84:87]\n\t"
        "ds_read_b64_tr_b16 v[16:17], %[wa0] offset:36864\n\t"
        "v_mfma_f32_16x16x32_bf16 v[88:91], v[32:35], v[40:43], v[88:91]\n\t"
        "ds_read_b64_tr_b16 v[18:19], %[wa0] offset:37632\n\t"
        "v_add_u32 v12, s40, %[px]\n\t"
        "v_mfma_f32_16x16x32_bf16 v[92:95], v[36:39], v[40:43], v[92:95]\n\t"
        "ds_read_b64_tr_b16 v[20:21], %[wa1] offset:36864\n\t"
        "s_add_u32 s40, s40, 1\n\t"
        "v_mfma_f32_16x16x32_bf16 v[104:107], v[36:39], v[44:47], v[104:107]\n\t"
        "ds_read_b128 v[68:71], v15 offset:8960\n\t"
        "v_lshlrev_b32 v12, 3, v12\n\t"
        "v_mfma_f32_16x16x32_bf16 v[100:103], v[32:35], v[44:47], v[100:103]\n\t"
        "ds_read_b64_tr_b16 v[22:23], %[wa1] offset:37632\n\t"
        "s_cmp_eq_u32 s40, 5\n\t"
        "v_mfma_f32_16x16x32_bf16 v[96:99], v[28:31], v[44:47], v[96:99]\n\t"
        "ds_read_b64_tr_b16 v[24:25], %[wa2] offset:36864\n\t"
        "v_bitop3_b32 v12, %[q16], v12, 32 bitop3:0x78\n\t"
        "v_mfma_f32_16x16x32_bf16 v[108:111], v[28:31], v[48:51], v[108:111]\n\t"
        "ds_read_b64_tr_b16 v[26:27], %[wa2] offset:37632\n\t"
        "s_cselect_b32 s42, 22784, 64\n\t"
        "v_mfma_f32_16x16x32_bf16 v[112:115], v[32:35], v[48:51], v[112:115]\n\t"
        "v_add3_u32 v14, v12, %[rb], s41\n\t"
        "v_mfma_f32_16x16x32_bf16 v[116:119], v[36:39], v[48:51], v[116:119]\n\t"
        "s_cselect_b32 s40, 0, s40\n\t"
        "v_mfma_f32_16x16x32_bf16 v[128:131], v[36:39], v[52:55], v[128:131]\n\t"
        "s_add_u32 s41, s41, s42\n\t"
        "v_mfma_f32_16x16x32_bf16 v[124:127], v[32:35], v[52:55], v[124:127]\n\t"
        "v_mfma_f32_16x16x32_bf16 v[120:123], v[28:31], v[52:55], v[120:123]\n\t"
        "v_mfma_f32_16x16x32_bf16 v[132:135], v[28:31], v[56:59], v[132:135]\n\t"
        "ds_read_b128 v[40:43], v14 offset:0\n\t"
        "v_mfma_f32_16x16x32_bf16 v[136:139], v[32:35], v[56:59], v[136:139]\n\t"
        "v_mfma_f32_16x16x32_bf16 v[140:143], v[36:39], v[56:59], v[140:143]\n\t"
        "v_mfma_f32_16x16x32_bf16 v[152:155], v[36:39], v[60:63], v[152:155]\n\t"
        "v_mfma_f32_16x16x32_bf16 v[148:151], v[32:35], v[60:63], v[148:151]\n\t"
        "v_mfma_f32_16x16x32_bf16 v[144:147], v[28:31], v[60:63], v[144:147]\n\t"
        "s_waitcnt lgkmcnt(8)\n\t"
        "v_mfma_f32_16x16x32_bf16 v[156:159], v[28:31], v[64:67], v[156:159]\n\t"
        "v_mfma_f32_16x16x32_bf16 v[160:163], v[32:35], v[64:67], v[160:163]\n\t"
        "s_waitcnt lgkmcnt(1)\n\t"
        "v_mfma_f32_16x16x32_bf16 v[164:167], v[36:39], v[64:67], v[164:167]\n\t"
        "v_mfma_f32_16x16x32_bf16 v[84:87], v[16:19], v[44:47], v[84:87]\n\t"
        "ds_read_b64_tr_b16 v[28:29], %[wa0] offset:43008\n\t"
        "v_mfma_f32_16x16x32_bf16 v[88:91], v[20:23], v[44:47], v[88:91]\n\t"
        "ds_read_b64_tr_b16 v[30:31], %[wa0] offset:43776\n\t"
        "v_mfma_f32_16x16x32_bf16 v[92:95], v[24:27], v[44:47], v[92:95]\n\t"
        "ds_read_b128 v[44:47], v14 offset:1280\n\t"
        "v_mfma_f32_16x16x32_bf16 v[104:107], v[24:27], v[48:51], v[104:107]\n\t"
        "ds_read_b128 v[72:75], v15 offset:10240\n\t"
        "v_mfma_f32_16x16x32_bf16 v[100:103], v[20:23], v[48:51], v[100:103]\n\t"
        "ds_read_b64_tr_b16 v[32:33], %[wa1] offset:43008\n\t"
        "v_mfma_f32_16x16x32_bf16 v[96:99], v[16:19], v[48:51], v[96:99]\n\t"
        "ds_read_b64_tr_b16 v[34:35], %[wa1] offset:43776\n\t"
        "v_mfma_f32_16x16x32_bf16 v[108:111], v[16:19], v[52:55], v[108:111]\n\t"
        "ds_read_b64_tr_b16 v[36:37], %[wa2] offset:43008\n\t"
        "v_mfma_f32_16x16x32_bf16 v[112:115], v[20:23], v[52:55], v[112:115]\n\t"
        "ds_read_b64_tr_b16 v[38:39], %[wa2] offset:43776\n\t"
        "v_mfma_f32_16x16x32_bf16 v[116:119], v[24:27], v[52:55], v[116:119]\n\t"
        "v_mfma_f32_16x16x32_bf16 v[128:131], v[24:27], v[56:59], v[128:131]\n\t"
        "v_mfma_f32_16x16x32_bf16 v[124:127], v[20:23], v[56:59], v[124:127]\n\t"
        "v_mfma_f32_16x16x32_bf16 v[120:123], v[16:19], v[56:59], v[120:123]\n\t"
        "v_mfma_f32_16x16x32_bf16 v[132:135], v[16:19], v[60:63], v[132:135]\n\t"
        "v_mfma_f32_16x16x32_bf16 v[136:139], v[20:23], v[60:63], v[136:139]\n\t"
        "v_mfma_f32_16x16x32_bf16 v[140:143], v[24:27], v[60:63], v[140:143]\n\t"
        "v_mfma_f32_16x16x32_bf16 v[152:155], v[24:27], v[64:67], v[152:155]\n\t"
        "v_mfma_f32_16x16x32_bf16 v[148:151], v[20:23], v[64:67], v[148:151]\n\t"
        "v_mfma_f32_16x16x32_bf16 v[144:147], v[16:19], v[64:67], v[144:147]\n\t"
        "v_mfma_f32_16x16x32_bf16 v[156:159], v[16:19], v[68:71], v[156:159]\n\t"
        "v_mfma_f32_16x16x32_bf16 v[160:163], v[20:23], v[68:71], v[160:163]\n\t"
        "s_waitcnt lgkmcnt(0)\n\t"
        "v_mfma_f32_16x16x32_bf16 v[164:167], v[24:27], v[68:71], v[164:167]\n\t"
        "v_mfma_f32_16x16x32_bf16 v[84:87], v[28:31], v[48:51], v[84:87]\n\t"
        "ds_read_b64_tr_b16 v[16:17], %[wa0] offset:49152\n\t"
        "v_mfma_f32_16x16x32_bf16 v[88:91], v[32:35], v[48:51], v[88:91]\n\t"
        "ds_read_b64_tr_b16 v[18:19], %[wa0] offset:49920\n\t"
        "v_mfma_f32_16x16x32_bf16 v[92:95], v[36:39], v[48:51], v[92:95]\n\t"
        "ds_read_b128 v[48:51], v14 offset:2560\n\t"
        "v_mfma_f32_16x16x32_bf16 v[104:107], v[36:39], v[52:55], v[104:107]\n\t"
        "ds_read_b128 v[76:79], v15 offset:11520\n\t"
        "v_mfma_f32_16x16x32_bf16 v[100:103], v[32:35], v[52:55], v[100:103]\n\t"
        "ds_read_b64_tr_b16 v[20:21], %[wa1] offset:49152\n\t"
        "v_mfma_f32_16x16x32_bf16 v[96:99], v[28:31], v[52:55], v[96:99]\n\t"
        "ds_read_b64_tr_b16 v[22:23], %[wa1] offset:49920\n\t"
        "v_mfma_f32_16x16x32_bf16 v[108:111], v[28:31], v[56:59], v[108:111]\n\t"
        "ds_read_b64_tr_b16 v[24:25], %[wa2] offset:49152\n\t"
        "v_mfma_f32_16x16x32_bf16 v[112:115], v[32:35], v[56:59], v[112:115]\n\t"
        "ds_read_b64_tr_b16 v[26:27], %[wa2] offset:49920\n\t"
        "v_mfma_f32_16x16x32_bf16 v[116:119], v[36:39], v[56:59], v[116:119]\n\t"
        "v_mfma_f32_16x16x32_bf16 v[128:131], v[36:39], v[60:63], v[128:131]\n\t"
        "v_mfma_f32_16x16x32_bf16 v[124:127], v[32:35], v[60:63], v[124:127]\n\t"
        "v_mfma_f32_16x16x32_bf16 v[120:123], v[28:31], v[60:63], v[120:123]\n\t"
        "v_mfma_f32_16x16x32_bf16 v[132:135], v[28:31], v[64:67], v[132:135]\n\t"
        "v_mfma_f32_16x16x32_bf16 v[136:139], v[32:35], v[64:67], v[136:139]\n\t"
        "v_mfma_f32_16x16x32_bf16 v[140:143], v[36:39], v[64:67], v[140:143]\n\t"
        "v_mfma_f32_16x16x32_bf16 v[152:155], v[36:39], v[68:71], v[152:155]\n\t"
        "v_mfma_f32_16x16x32_bf16 v[148:151], v[32:35], v[68:71], v[148:151]\n\t"
        "v_mfma_f32_16x16x32_bf16 v[144:147], v[28:31], v[68:71], v[144:147]\n\t"
        "v_mfma_f32_16x16x32_bf16 v[156:159], v[28:31], v[72:75], v[156:159]\n\t"
        "v_mfma_f32_16x16x32_bf16 v[160:163], v[32:35], v[72:75], v[160:163]\n\t"
        "s_waitcnt lgkmcnt(0)\n\t"
        "v_mfma_f32_16x16x32_bf16 v[164:167], v[36:39], v[72:75], v[164:167]\n\t"
        "v_mfma_f32_16x16x32_bf16 v[84:87], v[16:19], v[52:55], v[84:87]\n\t"
        "ds_read_b64_tr_b16 v[28:29], %[wa0] offset:55296\n\t"
        "v_mfma_f32_16x16x32_bf16 v[88:91], v[20:23], v[52:55], v[88:91]\n\t"
        "ds_read_b64_tr_b16 v[30:31], %[wa0] offset:56064\n\t"
        "v_mfma_f32_16x16x32_bf16 v[92:95], v[24:27], v[52:55], v[92:95]\n\t"
        "ds_read_b128 v[52:55], v14 offset:3840\n\t"
        "v_mfma_f32_16x16x32_bf16 v[104:107], v[24:27], v[56:59], v[104:107]\n\t"
        "ds_read_b128 v[80:83], v15 offset:12800\n\t"
        "v_mfma_f32_16x16x32_bf16 v[100:103], v[20:23], v[56:59], v[100:103]\n\t"
        "ds_read_b64_tr_b16 v[32:33], %[wa1] offset:55296\n\t"
        "v_mfma_f32_16x16x32_bf16 v[96:99], v[16:19], v[56:59], v[96:99]\n\t"
        "ds_read_b64_tr_b16 v[34:35], %[wa1] offset:56064\n\t"
        "v_mfma_f32_16x16x32_bf16 v[108:111], v[16:19], v[60:63], v[108:111]\n\t"
        "ds_read_b64_tr_b16 v[36:37], %[wa2] offset:55296\n\t"
        "v_mfma_f32_16x16x32_bf16 v[112:115], v[20:23], v[60:63], v[112:115]\n\t"
        "ds_read_b64_tr_b16 v[38:39], %[wa2] offset:56064\n\t"
        "v_mfma_f32_16x16x32_bf16 v[116:119], v[24:27], v[60:63], v[116:119]\n\t"
        "v_mfma_f32_16x16x32_bf16 v[128:131], v[24:27], v[64:67], v[128:131]\n\t"
        "v_mfma_f32_16x16x32_bf16 v[124:127], v[20:23], v[64:67], v[124:127]\n\t"
        "v_mfma_f32_16x16x32_bf16 v[120:123], v[16:19], v[64:67], v[120:123]\n\t"
        "v_mfma_f32_16x16x32_bf16 v[132:135], v[16:19], v[68:71], v[132:135]\n\t"
        "v_mfma_f32_16x16x32_bf16 v[136:139], v[20:23], v[68:71], v[136:139]\n\t"
        "v_mfma_f32_16x16x32_bf16 v[140:143], v[24:27], v[68:71], v[140:143]\n\t"
        "v_mfma_f32_16x16x32_bf16 v[152:155], v[24:27], v[72:75], v[152:155]\n\t"
        "v_mfma_f32_16x16x32_bf16 v[148:151], v[20:23], v[72:75], v[148:151]\n\t"
        "v_mfma_f32_16x16x32_bf16 v[144:147], v[16:19], v[72:75], v[144:147]\n\t"
        "v_mfma_f32_16x16x32_bf16 v[156:159], v[16:19], v[76:79], v[156:159]\n\t"
        "v_mfma_f32_16x16x32_bf16 v[160:163], v[20:23], v[76:79], v[160:163]\n\t"
        "v_mfma_f32_16x16x32_bf16 v[164:167], v[24:27], v[76:79], v[164:167]\n\t"
        "s_waitcnt lgkmcnt(0)\n\t"
        "s_barrier\n\t"
        "v_mfma_f32_16x16x32_bf16 v[84:87], v[28:31], v[56:59], v[84:87]\n\t"
        "ds_read_b64_tr_b16 v[16:17], %[wa0] offset:0\n\t"
        "v_mfma_f32_16x16x32_bf16 v[88:91], v[32:35], v[56:59], v[88:91]\n\t"
        "ds_read_b64_tr_b16 v[18:19], %[wa0] offset:768\n\t"
        "v_mfma_f32_16x16x32_bf16 v[92:95], v[36:39], v[56:59], v[92:95]\n\t"
        "ds_read_b128 v[56:59], v14 offset:5120\n\t"
        "v_mfma_f32_16x16x32_bf16 v[104:107], v[36:39], v[60:63], v[104:107]\n\t"
        "ds_read_b64_tr_b16 v[20:21], %[wa1] offset:0\n\t"
        "v_mfma_f32_16x16x32_bf16 v[100:103], v[32:35], v[60:63], v[100:103]\n\t"
        "ds_read_b64_tr_b16 v[22:23], %[wa1] offset:768\n\t"
        "v_mfma_f32_16x16x32_bf16 v[96:99], v[28:31], v[60:63], v[96:99]\n\t"
        "ds_read_b128 v[60:63], v14 offset:6400\n\t"
        "v_mfma_f32_16x16x32_bf16 v[108:111], v[28:31], v[64:67], v[108:111]\n\t"
        "ds_read_b64_tr_b16 v[24:25], %[wa2] offset:0\n\t"
        "v_mfma_f32_16x16x32_bf16 v[112:115], v[32:35], v[64:67], v[112:115]\n\t"
        "ds_read_b64_tr_b16 v[26:27], %[wa2] offset:768\n\t"
        "v_mfma_f32_16x16x32_bf16 v[116:119], v[36:39], v[64:67], v[116:119]\n\t"
        "ds_read_b128 v[64:67], v14 offset:7680\n\t"
        "v_mfma_f32_16x16x32_bf16 v[128:131], v[36:39], v[68:71], v[128:131]\n\t"
        "ds_read_b32 v13, %[ca] offset:112\n\t"
        "v_mfma_f32_16x16x32_bf16 v[124:127], v[32:35], v[68:71], v[124:127]\n\t"
        "v_mfma_f32_16x16x32_bf16 v[120:123], v[28:31], v[68:71], v[120:123]\n\t"
        "v_mfma_f32_16x16x32_bf16 v[132:135], v[28:31], v[72:75], v[132:135]\n\t"
        "v_mfma_f32_16x16x32_bf16 v[136:139], v[32:35], v[72:75], v[136:139]\n\t"
        "v_mfma_f32_16x16x32_bf16 v[140:143], v[36:39], v[72:75], v[140:143]\n\t"
        "s_waitcnt lgkmcnt(0)\n\t"
        "v_cmp_ne_u32 vcc, 0, v13\n\t"
        "v_mfma_f32_16x16x32_bf16 v[152:155], v[36:39], v[76:79], v[152:155]\n\t"
        "v_mfma_f32_16x16x32_bf16 v[148:151], v[32:35], v[76:79], v[148:151]\n\t"
        "s_cbranch_vccnz 2f\n\t"
        "v_mfma_f32_16x16x32_bf16 v[144:147], v[28:31], v[76:79], v[144:147]\n\t"
        "v_mfma_f32_16x16x32_bf16 v[156:159], v[28:31], v[80:83], v[156:159]\n\t"
        "v_mfma_f32_16x16x32_bf16 v[160:163], v[32:35], v[80:83], v[160:163]\n\t"
        "v_mfma_f32_16x16x32_bf16 v[164:167], v[36:39], v[80:83], v[164:167]\n\t"
        "s_branch 80b\n\t"
        "81:\n\t"
        "s_waitcnt lgkmcnt(0)\n\t"
        "v_mfma_f32_16x16x32_bf16 v[84:87], v[28:31], v[40:43], v[84:87]\n\t"
        "v_add_u32 v12, s40, %[px]\n\t"
        "ds_read_b64_tr_b16 v[16:17], %[wa0] offset:36864\n\t"
        "v_mfma_f32_16x16x32_bf16 v[88:91], v[32:35], v[40:43], v[88:91]\n\t"
        "s_add_u32 s40, s40, 1\n\t"
        "ds_read_b64_tr_b16 v[18:19], %[wa0] offset:37632\n\t"
        "v_mfma_f32_16x16x32_bf16 v[92:95], v[36:39], v[40:43], v[92:95]\n\t"
        "v_lshlrev_b32 v12, 3, v12\n\t"
        "ds_read_b64_tr_b16 v[20:21], %[wa1] offset:36864\n\t"
        "v_mfma_f32_16x16x32_bf16 v[104:107], v[36:39], v[44:47], v[104:107]\n\t"
        "s_cmp_eq_u32 s40, 5\n\t"
        "ds_read_b64_tr_b16 v[22:23], %[wa1] offset:37632\n\t"
        "v_mfma_f32_16x16x32_bf16 v[100:103], v[32:35], v[44:47], v[100:103]\n\t"
        "v_bitop3_b32 v12, %[q16], v12, 32 bitop3:0x78\n\t"
        "ds_read_b64_tr_b16 v[24:25], %[wa2] offset:36864\n\t"
        "v_mfma_f32_16x16x32_bf16 v[96:99], v[28:31], v[44:47], v[96:99]\n\t"
        "s_cselect_b32 s42, 22784, 64\n\t"
        "ds_read_b64_tr_b16 v[26:27], %[wa2] offset:37632\n\t"
        "v_mfma_f32_16x16x32_bf16 v[108:111], v[28:31], v[48:51], v[108:111]\n\t"
        "v_add3_u32 v14, v12, %[rb], s41\n\t"
        "v_mfma_f32_16x16x32_bf16 v[112:115], v[32:35], v[48:51], v[112:115]\n\t"
        "s_cselect_b32 s40, 0, s40\n\t"
        "v_mfma_f32_16x16x32_bf16 v[116:119], v[36:39], v[48:51], v[116:119]\n\t"
        "s_add_u32 s41, s41, s42\n\t"
        "v_mfma_f32_16x16x32_bf16 v[128:131], v[36:39], v[52:55], v[128:131]\n\t"
        "v_mfma_f32_16x16x32_bf16 v[124:127], v[32:35], v[52:55], v[124:127]\n\t"
        "v_mfma_f32_16x16x32_bf16 v[120:123], v[28:31], v[52:55], v[120:123]\n\t"
        "ds_read_b64_tr_b16 v[28:29], %[wa0] offset:43008\n\t"
        "s_waitcnt lgkmcnt(1)\n\t"
        "v_mfma_f32_16x16x32_bf16 v[84:87], v[16:19], v[44:47], v[84:87]\n\t"
        "ds_read_b64_tr_b16 v[30:31], %[wa0] offset:43776\n\t"
        "v_mfma_f32_16x16x32_bf16 v[88:91], v[20:23], v[44:47], v[88:91]\n\t"
        "ds_read_b64_tr_b16 v[32:33], %[wa1] offset:43008\n\t"
        "v_mfma_f32_16x16x32_bf16 v[92:95], v[24:27], v[44:47], v[92:95]\n\t"
        "ds_read_b64_tr_b16 v[34:35], %[wa1] offset:43776\n\t"
        "v_mfma_f32_16x16x32_bf16 v[104:107], v[24:27], v[48:51], v[104:107]\n\t"
        "ds_read_b64_tr_b16 v[36:37], %[wa2] offset:43008\n\t"
        "v_mfma_f32_16x16x32_bf16 v[100:103], v[20:23], v[48:51], v[100:103]\n\t"
        "ds_read_b64_tr_b16 v[38:39], %[wa2] offset:43776\n\t"
        "v_mfma_f32_16x16x32_bf16 v[96:99], v[16:19], v[48:51], v[96:99]\n\t"
        "v_mfma_f32_16x16x32_bf16 v[108:111], v[16:19], v[52:55], v[108:111]\n\t"
        "v_mfma_f32_16x16x32_bf16 v[112:115], v[20:23], v[52:55], v[112:115]\n\t"
        "v_mfma_f32_16x16x32_bf16 v[116:119], v[24:27], v[52:55], v[116:119]\n\t"
        "v_mfma_f32_16x16x32_bf16 v[128:131], v[24:27], v[56:59], v[128:131]\n\t"
        "v_mfma_f32_16x16x32_bf16 v[124:127], v[20:23], v[56:59], v[124:127]\n\t"
        "v_mfma_f32_16x16x32_bf16 v[120:123], v[16:19], v[56:59], v[120:123]\n\t"
        "ds_read_b64_tr_b16 v[16:17], %[wa0] offset:49152\n\t"
        "s_waitcnt lgkmcnt(1)\n\t"
        "v_mfma_f32_16x16x32_bf16 v[84:87], v[28:31], v[48:51], v[84:87]\n\t"
        "ds_read_b64_tr_b16 v[18:19], %[wa0] offset:49920\n\t"
        "v_mfma_f32_16x16x32_bf16 v[88:91], v[32:35], v[48:51], v[88:91]\n\t"
        "ds_read_b64_tr_b16 v[20:21], %[wa1] offset:49152\n\t"
        "v_mfma_f32_16x16x32_bf16 v[92:95], v[36:39], v[48:51], v[92:95]\n\t"
        "ds_read_b64_tr_b16 v[22:23], %[wa1] offset:49920\n\t"
        "v_mfma_f32_16x16x32_bf16 v[104:107], v[36:39], v[52:55], v[104:107]\n\t"
        "ds_read_b64_tr_b16 v[24:25], %[wa2] offset:49152\n\t"
        "v_mfma_f32_16x16x32_bf16 v[100:103], v[32:35], v[52:55], v[100:103]\n\t"
        "ds_read_b64_tr_b16 v[26:27], %[wa2] offset:49920\n\t"
        "v_mfma_f32_16x16x32_bf16 v[96:99], v[28:31], v[52:55], v[96:99]\n\t"
        "v_mfma_f32_16x16x32_bf16 v[108:111], v[28:31], v[56:59], v[108:111]\n\t"
        "v_mfma_f32_16x16x32_bf16 v[112:115], v[32:35], v[56:59], v[112:115]\n\t"
        "v_mfma_f32_16x16x32_bf16 v[116:119], v[36:39], v[56:59], v[116:119]\n\t"
        "v_mfma_f32_16x16x32_bf16 v[128:131], v[36:39], v[60:63], v[128:131]\n\t"
        "ds_read_b128 v[68:71], v15 offset:8960\n\t"
        "v_mfma_f32_16x16x32_bf16 v[124:127], v[32:35], v[60:63], v[124:127]\n\t"
        "v_mfma_f32_16x16x32_bf16 v[120:123], v[28:31], v[60:63], v[120:123]\n\t"
        "ds_read_b64_tr_b16 v[28:29], %[wa0] offset:55296\n\t"
        "s_waitcnt lgkmcnt(2)\n\t"
        "v_mfma_f32_16x16x32_bf16 v[84:87], v[16:19], v[52:55], v[84:87]\n\t"
        "ds_read_b64_tr_b16 v[30:31], %[wa0] offset:56064\n\t"
        "v_mfma_f32_16x16x32_bf16 v[88:91], v[20:23], v[52:55], v[88:91]\n\t"
        "ds_read_b64_tr_b16 v[32:33], %[wa1] offset:55296\n\t"
        "v_mfma_f32_16x16x32_bf16 v[92:95], v[24:27], v[52:55], v[92:95]\n\t"
        "ds_read_b64_tr_b16 v[34:35], %[wa1] offset:56064\n\t"
        "v_mfma_f32_16x16x32_bf16 v[104:107], v[24:27], v[56:59], v[104:107]\n\t"
        "ds_read_b64_tr_b16 v[36:37], %[wa2] offset:55296\n\t"
        "v_mfma_f32_16x16x32_bf16 v[100:103], v[20:23], v[56:59], v[100:103]\n\t"
        "ds_read_b64_tr_b16 v[38:39], %[wa2] offset:56064\n\t"
        "v_mfma_f32_16x16x32_bf16 v[96:99], v[16:19], v[56:59], v[96:99]\n\t"
        "ds_read_b128 v[40:43], v14 offset:0\n\t"
        "v_mfma_f32_16x16x32_bf16 v[108:111], v[16:19], v[60:63], v[108:111]\n\t"
        "ds_read_b128 v[44:47], v14 offset:1280\n\t"
        "v_mfma_f32_16x16x32_bf16 v[112:115], v[20:23], v[60:63], v[112:115]\n\t"
        "ds_read_b128 v[48:51], v14 offset:2560\n\t"
        "v_mfma_f32_16x16x32_bf16 v[116:119], v[24:27], v[60:63], v[116:119]\n\t"
        "v_mfma_f32_16x16x32_bf16 v[128:131], v[24:27], v[64:67], v[128:131]\n\t"
        "ds_read_b128 v[52:55], v14 offset:3840\n\t"
        "v_mfma_f32_16x16x32_bf16 v[124:127], v[20:23], v[64:67], v[124:127]\n\t"
        "v_mfma_f32_16x16x32_bf16 v[120:123], v[16:19], v[64:67], v[120:123]\n\t"
        "s_waitcnt lgkmcnt(4)\n\t"
        "s_barrier\n\t"
        "v_mfma_f32_16x16x32_bf16 v[84:87], v[28:31], v[56:59], v[84:87]\n\t"
        "ds_read_b64_tr_b16 v[16:17], %[wa0] offset:0\n\t"
        "v_mfma_f32_16x16x32_bf16 v[88:91], v[32:35], v[56:59], v[88:91]\n\t"
        "ds_read_b64_tr_b16 v[18:19], %[wa0] offset:768\n\t"
        "v_mfma_f32_16x16x32_bf16 v[92:95], v[36:39], v[56:59], v[92:95]\n\t"
        "ds_read_b64_tr_b16 v[20:21], %[wa1] offset:0\n\t"
        "v_mfma_f32_16x16x32_bf16 v[104:107], v[36:39], v[60:63], v[104:107]\n\t"
        "ds_read_b64_tr_b16 v[22:23], %[wa1] offset:768\n\t"
        "v_mfma_f32_16x16x32_bf16 v[100:103], v[32:35], v[60:63], v[100:103]\n\t"
        "ds_read_b64_tr_b16 v[24:25], %[wa2] offset:0\n\t"
        "v_mfma_f32_16x16x32_bf16 v[96:99], v[28:31], v[60:63], v[96:99]\n\t"
        "ds_read_b64_tr_b16 v[26:27], %[wa2] offset:768\n\t"
        "v_mfma_f32_16x16x32_bf16 v[108:111], v[28:31], v[64:67], v[108:111]\n\t"
        "v_mfma_f32_16x16x32_bf16 v[112:115], v[32:35], v[64:67], v[112:115]\n\t"
        "v_mfma_f32_16x16x32_bf16 v[116:119], v[36:39], v[64:67], v[116:119]\n\t"
        "v_mfma_f32_16x16x32_bf16 v[128:131], v[36:39], v[68:71], v[128:131]\n\t"
        "ds_read_b128 v[56:59], v14 offset:5120\n\t"
        "v_mfma_f32_16x16x32_bf16 v[124:127], v[32:35], v[68:71], v[124:127]\n\t"
        "v_mfma_f32_16x16x32_bf16 v[120:123], v[28:31], v[68:71], v[120:123]\n\t"
        "ds_read_b64_tr_b16 v[28:29], %[wa0] offset:6144\n\t"
        "s_waitcnt lgkmcnt(2)\n\t"
        "v_mfma_f32_16x16x32_bf16 v[84:87], v[16:19], v[40:43], v[84:87]\n\t"
        "ds_read_b64_tr_b16 v[30:31], %[wa0] offset:6912\n\t"
        "v_mfma_f32_16x16x32_bf16 v[88:91], v[20:23], v[40:43], v[88:91]\n\t"
        "ds_read_b64_tr_b16 v[32:33], %[wa1] offset:6144\n\t"
        "v_mfma_f32_16x16x32_bf16 v[92:95], v[24:27], v[40:43], v[92:95]\n\t"
        "ds_read_b64_tr_b16 v[34:35], %[wa1] offset:6912\n\t"
        "v_mfma_f32_16x16x32_bf16 v[104:107], v[24:27], v[44:47], v[104:107]\n\t"
        "ds_read_b64_tr_b16 v[36:37], %[wa2] offset:6144\n\t"
        "v_mfma_f32_16x16x32_bf16 v[100:103], v[20:23], v[44:47], v[100:103]\n\t"
        "ds_read_b64_tr_b16 v[38:39], %[wa2] offset:6912\n\t"
        "v_mfma_f32_16x16x32_bf16 v[96:99], v[16:19], v[44:47], v[96:99]\n\t"
        "v_mfma_f32_16x16x32_bf16 v[108:111], v[16:19], v[48:51], v[108:111]\n\t"
        "v_mfma_f32_16x16x32_bf16 v[112:115], v[20:23], v[48:51], v[112:115]\n\t"
        "v_mfma_f32_16x16x32_bf16 v[116:119], v[24:27], v[48:51], v[116:119]\n\t"
        "v_mfma_f32_16x16x32_bf16 v[128:131], v[24:27], v[52:55], v[128:131]\n\t"
        "ds_read_b128 v[60:63], v14 offset:6400\n\t"
        "v_mfma_f32_16x16x32_bf16 v[124:127], v[20:23], v[52:55], v[124:127]\n\t"
        "v_mfma_f32_16x16x32_bf16 v[120:123], v[16:19], v[52:55], v[120:123]\n\t"
        "ds_read_b64_tr_b16 v[16:17], %[wa0] offset:12288\n\t"
        "s_waitcnt lgkmcnt(2)\n\t"
        "v_mfma_f32_16x16x32_bf16 v[84:87], v[28:31], v[44:47], v[84:87]\n\t"
        "ds_read_b64_tr_b16 v[18:19], %[wa0] offset:13056\n\t"
        "v_lshlrev_b32 v12, 2, %[ta]\n\t"
        "v_mfma_f32_16x16x32_bf16 v[88:91], v[32:35], v[44:47], v[88:91]\n\t"
        "ds_read_b64_tr_b16 v[20:21], %[wa1] offset:12288\n\t"
        "v_lshrrev_b32 v13, 1, %[ta]\n\t"
        "v_mfma_f32_16x16x32_bf16 v[92:95], v[36:39], v[44:47], v[92:95]\n\t"
        "ds_read_b64_tr_b16 v[22:23], %[wa1] offset:13056\n\t"
        "v_and_b32 v12, 0x3c0, v12\n\t"
        "v_mfma_f32_16x16x32_bf16 v[104:107], v[36:39], v[48:51], v[104:107]\n\t"
        "ds_read_b64_tr_b16 v[24:25], %[wa2] offset:12288\n\t"
        "v_and_or_b32 v12, v13, 48, v12\n\t"
        "v_mfma_f32_16x16x32_bf16 v[100:103], v[32:35], v[48:51], v[100:103]\n\t"
        "ds_read_b64_tr_b16 v[26:27], %[wa2] offset:13056\n\t"
        "v_and_b32 v13, 48, %[vo2]\n\t"
        "v_mfma_f32_16x16x32_bf16 v[96:99], v[28:31], v[48:51], v[96:99]\n\t"
        "v_xor_b32 v13, v12, v13\n\t"
        "v_mfma_f32_16x16x32_bf16 v[108:111], v[28:31], v[52:55], v[108:111]\n\t"
        "s_lshl_b32 s40, %[nh], 10\n\t"
        "v_mfma_f32_16x16x32_bf16 v[112:115], v[32:35], v[52:55], v[112:115]\n\t"
        "s_add_u32 s40, s40, %[xt]\n\t"
        "v_mfma_f32_16x16x32_bf16 v[116:119], v[36:39], v[52:55], v[116:119]\n\t"
        "v_add_u32 v13, s40, v13\n\t"
        "v_mfma_f32_16x16x32_bf16 v[128:131], v[36:39], v[56:59], v[128:131]\n\t"
        "ds_read_b128 v[64:67], v14 offset:7680\n\t"
        "s_lshl_b32 s40, %[nh], 5\n\t"
        "v_mfma_f32_16x16x32_bf16 v[124:127], v[32:35], v[56:59], v[124:127]\n\t"
        "v_xor_b32 v12, s40, v12\n\t"
        "v_mfma_f32_16x16x32_bf16 v[120:123], v[28:31], v[56:59], v[120:123]\n\t"
        "ds_read_b64_tr_b16 v[28:29], %[wa0] offset:18432\n\t"
        "v_lshrrev_b32 %[ca], 1, %[q16]\n\t"
        "s_waitcnt lgkmcnt(2)\n\t"
        "v_mfma_f32_16x16x32_bf16 v[84:87], v[16:19], v[48:51], v[84:87]\n\t"
        "ds_read_b64_tr_b16 v[30:31], %[wa0] offset:19200\n\t"
        "v_xor_b32 v12, %[ca], v12\n\t"
        "v_mfma_f32_16x16x32_bf16 v[88:91], v[20:23], v[48:51], v[88:91]\n\t"
        "ds_read_b64_tr_b16 v[32:33], %[wa1] offset:18432\n\t"
        "v_add_u32 v12, %[xt], v12\n\t"
        "v_mfma_f32_16x16x32_bf16 v[92:95], v[24:27], v[48:51], v[92:95]\n\t"
        "ds_read_b64_tr_b16 v[34:35], %[wa1] offset:19200\n\t"
        "v_mfma_f32_16x16x32_bf16 v[104:107], v[24:27], v[52:55], v[104:107]\n\t"
        "ds_read_b64_tr_b16 v[36:37], %[wa2] offset:18432\n\t"
        "v_mfma_f32_16x16x32_bf16 v[100:103], v[20:23], v[52:55], v[100:103]\n\t"
        "ds_read_b64_tr_b16 v[38:39], %[wa2] offset:19200\n\t"
        "v_mfma_f32_16x16x32_bf16 v[96:99], v[16:19], v[52:55], v[96:99]\n\t"
        "v_mfma_f32_16x16x32_bf16 v[108:111], v[16:19], v[56:59], v[108:111]\n\t"
        "v_mfma_f32_16x16x32_bf16 v[112:115], v[20:23], v[56:59], v[112:115]\n\t"
        "v_mfma_f32_16x16x32_bf16 v[116:119], v[24:27], v[56:59], v[116:119]\n\t"
        "v_mfma_f32_16x16x32_bf16 v[128:131], v[24:27], v[60:63], v[128:131]\n\t"
        "ds_read_b128 v[68:71], v14 offset:8960\n\t"
        "v_mfma_f32_16x16x32_bf16 v[124:127], v[20:23], v[60:63], v[124:127]\n\t"
        "v_mfma_f32_16x16x32_bf16 v[120:123], v[16:19], v[60:63], v[120:123]\n\t"
        "ds_read_b64_tr_b16 v[16:17], %[wa0] offset:24576\n\t"
        "s_waitcnt lgkmcnt(2)\n\t"
        "v_mfma_f32_16x16x32_bf16 v[84:87], v[28:31], v[52:55], v[84:87]\n\t"
        "ds_read_b64_tr_b16 v[18:19], %[wa0] offset:25344\n\t"
        "v_mfma_f32_16x16x32_bf16 v[88:91], v[32:35], v[52:55], v[88:91]\n\t"
        "ds_read_b64_tr_b16 v[20:21], %[wa1] offset:24576\n\t"
        "v_mfma_f32_16x16x32_bf16 v[92:95], v[36:39], v[52:55], v[92:95]\n\t"
        "ds_read_b64_tr_b16 v[22:23], %[wa1] offset:25344\n\t"
        "v_mfma_f32_16x16x32_bf16 v[104:107], v[36:39], v[56:59], v[104:107]\n\t"
        "ds_read_b64_tr_b16 v[24:25], %[wa2] offset:24576\n\t"
        "v_mfma_f32_16x16x32_bf16 v[100:103], v[32:35], v[56:59], v[100:103]\n\t"
        "ds_read_b64_tr_b16 v[26:27], %[wa2] offset:25344\n\t"
        "v_mfma_f32_16x16x32_bf16 v[96:99], v[28:31], v[56:59], v[96:99]\n\t"
        "v_mfma_f32_16x16x32_bf16 v[108:111], v[28:31], v[60:63], v[108:111]\n\t"
        "v_mfma_f32_16x16x32_bf16 v[112:115], v[32:35], v[60:63], v[112:115]\n\t"
        "v_mfma_f32_16x16x32_bf16 v[116:119], v[36:39], v[60:63], v[116:119]\n\t"
        "v_mfma_f32_16x16x32_bf16 v[128:131], v[36:39], v[64:67], v[128:131]\n\t"
        "v_mfma_f32_16x16x32_bf16 v[124:127], v[32:35], v[64:67], v[124:127]\n\t"
        "v_mfma_f32_16x16x32_bf16 v[120:123], v[28:31], v[64:67], v[120:123]\n\t"
        "ds_read_b64_tr_b16 v[28:29], %[wa0] offset:0\n\t"
        "s_waitcnt lgkmcnt(1)\n\t"
        "v_mfma_f32_16x16x32_bf16 v[84:87], v[16:19], v[56:59], v[84:87]\n\t"
        "ds_read_b64_tr_b16 v[30:31], %[wa0] offset:768\n\t"
        "v_mfma_f32_16x16x32_bf16 v[88:91], v[20:23], v[56:59], v[88:91]\n\t"
        "ds_read_b64_tr_b16 v[32:33], %[wa1] offset:0\n\t"
        "v_mfma_f32_16x16x32_bf16 v[92:95], v[24:27], v[56:59], v[92:95]\n\t"
        "ds_read_b64_tr_b16 v[34:35], %[wa1] offset:768\n\t"
        "v_mfma_f32_16x16x32_bf16 v[104:107], v[24:27], v[60:63], v[104:107]\n\t"
        "ds_read_b64_tr_b16 v[36:37], %[wa2] offset:0\n\t"
        "v_mfma_f32_16x16x32_bf16 v[100:103], v[20:23], v[60:63], v[100:103]\n\t"
        "ds_read_b64_tr_b16 v[38:39], %[wa2] offset:768\n\t"
        "v_mfma_f32_16x16x32_bf16 v[96:99], v[16:19], v[60:63], v[96:99]\n\t"
        "v_mfma_f32_16x16x32_bf16 v[108:111], v[16:19], v[64:67], v[108:111]\n\t"
        "v_mfma_f32_16x16x32_bf16 v[112:115], v[20:23], v[64:67], v[112:115]\n\t"
        "v_mfma_f32_16x16x32_bf16 v[116:119], v[24:27], v[64:67], v[116:119]\n\t"
        "v_mfma_f32_16x16x32_bf16 v[128:131], v[24:27], v[68:71], v[128:131]\n\t"
        "v_mfma_f32_16x16x32_bf16 v[124:127], v[20:23], v[68:71], v[124:127]\n\t"
        "v_mfma_f32_16x16x32_bf16 v[120:123], v[16:19], v[68:71], v[120:123]\n\t"
        "ds_read_b64_tr_b16 v[16:17], %[wa0] offset:6144\n\t"
        "s_waitcnt lgkmcnt(3)\n\t"
        "v_mfma_f32_16x16x32_bf16 v[132:135], v[28:31], v[56:59], v[132:135]\n\t"
        "ds_read_b64_tr_b16 v[18:19], %[wa0] offset:6912\n\t"
        "v_mfma_f32_16x16x32_bf16 v[136:139], v[32:35], v[56:59], v[136:139]\n\t"
        "ds_read_b64_tr_b16 v[20:21], %[wa1] offset:6144\n\t"
        "v_cvt_pk_bf16_f32 v84, v84, v85\n\t"
        "s_waitcnt lgkmcnt(3)\n\t"
        "v_mfma_f32_16x16x32_bf16 v[140:143], v[36:39], v[56:59], v[140:143]\n\t"
        "ds_read_b64_tr_b16 v[22:23], %[wa1] offset:6912\n\t"
        "v_cvt_pk_bf16_f32 v85, v86, v87\n\t"
        "v_mfma_f32_16x16x32_bf16 v[152:155], v[36:39], v[60:63], v[152:155]\n\t"
        "ds_read_b64_tr_b16 v[24:25], %[wa2] offset:6144\n\t"
        "v_cvt_pk_bf16_f32 v86, v88, v89\n\t"
        "v_mfma_f32_16x16x32_bf16 v[148:151], v[32:35], v[60:63], v[148:151]\n\t"
        "ds_read_b64_tr_b16 v[26:27], %[wa2] offset:6912\n\t"
        "v_cvt_pk_bf16_f32 v87, v90, v91\n\t"
        "v_mfma_f32_16x16x32_bf16 v[144:147], v[28:31], v[60:63], v[144:147]\n\t"
        "ds_read_b128 v[72:75], v14 offset:10240\n\t"
        "s_mov_b32 s40, 0\n\t"
        "v_mfma_f32_16x16x32_bf16 v[156:159], v[28:31], v[64:67], v[156:159]\n\t"
        "s_nop 0\n\t"
        "v_permlane16_swap_b32 v84, v86\n\t"
        "v_mfma_f32_16x16x32_bf16 v[160:163], v[32:35], v[64:67], v[160:163]\n\t"
        "v_permlane16_swap_b32 v85, v87\n\t"
        "buffer_store_dwordx4 v[84:87], %[vo], %[ro], s40 offen sc1\n\t"
        "v_mfma_f32_16x16x32_bf16 v[164:167], v[36:39], v[64:67], v[164:167]\n\t"
        "ds_read_b64_tr_b16 v[28:29], %[wa0] offset:12288\n\t"
        "v_cvt_pk_bf16_f32 v92, v92, v93\n\t"
        "s_waitcnt lgkmcnt(4)\n\t"
        "v_mfma_f32_16x16x32_bf16 v[132:135], v[16:19], v[60:63], v[132:135]\n\t"
        "ds_read_b64_tr_b16 v[30:31], %[wa0] offset:13056\n\t"
        "v_cvt_pk_bf16_f32 v93, v94, v95\n\t"
        "v_mfma_f32_16x16x32_bf16 v[136:139], v[20:23], v[60:63], v[136:139]\n\t"
        "ds_read_b64_tr_b16 v[32:33], %[wa1] offset:12288\n\t"
        "ds_write_b64 v12, v[92:93] offset:0\n\t"
        "s_waitcnt lgkmcnt(5)\n\t"
        "v_mfma_f32_16x16x32_bf16 v[140:143], v[24:27], v[60:63], v[140:143]\n\t"
        "ds_read_b64_tr_b16 v[34:35], %[wa1] offset:13056\n\t"
        "v_cvt_pk_bf16_f32 v104, v104, v105\n\t"
        "v_mfma_f32_16x16x32_bf16 v[152:155], v[24:27], v[64:67], v[152:155]\n\t"
        "ds_read_b64_tr_b16 v[36:37], %[wa2] offset:12288\n\t"
        "v_cvt_pk_bf16_f32 v105, v106, v107\n\t"
        "v_mfma_f32_16x16x32_bf16 v[148:151], v[20:23], v[64:67], v[148:151]\n\t"
        "ds_read_b64_tr_b16 v[38:39], %[wa2] offset:13056\n\t"
        "ds_write_b64 v12, v[104:105] offset:1024\n\t"
        "v_mfma_f32_16x16x32_bf16 v[144:147], v[16:19], v[64:67], v[144:147]\n\t"
        "ds_read_b128 v[76:79], v14 offset:11520\n\t"
        "v_cvt_pk_bf16_f32 v96, v96, v97\n\t"
        "v_mfma_f32_16x16x32_bf16 v[156:159], v[16:19], v[68:71], v[156:159]\n\t"
        "v_cvt_pk_bf16_f32 v97, v98, v99\n\t"
        "v_cvt_pk_bf16_f32 v98, v100, v101\n\t"
        "v_mfma_f32_16x16x32_bf16 v[160:163], v[20:23], v[68:71], v[160:163]\n\t"
        "v_cvt_pk_bf16_f32 v99, v102, v103\n\t"
        "s_mov_b32 s40, 43008\n\t"
        "v_mfma_f32_16x16x32_bf16 v[164:167], v[24:27], v[68:71], v[164:167]\n\t"
        "ds_read_b64_tr_b16 v[16:17], %[wa0] offset:18432\n\t"
        "s_nop 0\n\t"
        "s_waitcnt lgkmcnt(5)\n\t"
        "v_mfma_f32_16x16x32_bf16 v[132:135], v[28:31], v[64:67], v[132:135]\n\t"
        "ds_read_b64_tr_b16 v[18:19], %[wa0] offset:19200\n\t"
        "v_permlane16_swap_b32 v96, v98\n\t"
        "v_mfma_f32_16x16x32_bf16 v[136:139], v[32:35], v[64:67], v[136:139]\n\t"
        "ds_read_b64_tr_b16 v[20:21], %[wa1] offset:18432\n\t"
        "v_permlane16_swap_b32 v97, v99\n\t"
        "s_waitcnt lgkmcnt(5)\n\t"
        "v_mfma_f32_16x16x32_bf16 v[140:143], v[36:39], v[64:67], v[140:143]\n\t"
        "ds_read_b64_tr_b16 v[22:23], %[wa1] offset:19200\n\t"
        "buffer_store_dwordx4 v[96:99], %[vo], %[ro], s40 offen sc1\n\t"
        "v_mfma_f32_16x16x32_bf16 v[152:155], v[36:39], v[68:71], v[152:155]\n\t"
        "ds_read_b64_tr_b16 v[24:25], %[wa2] offset:18432\n\t"
        "v_cvt_pk_bf16_f32 v116, v116, v117\n\t"
        "v_mfma_f32_16x16x32_bf16 v[148:151], v[32:35], v[68:71], v[148:151]\n\t"
        "ds_read_b64_tr_b16 v[26:27], %[wa2] offset:19200\n\t"
        "v_cvt_pk_bf16_f32 v117, v118, v119\n\t"
        "v_mfma_f32_16x16x32_bf16 v[144:147], v[28:31], v[68:71], v[144:147]\n\t"
        "ds_read_b128 v[80:83], v14 offset:12800\n\t"
        "ds_write_b64 v12, v[116:117] offset:2048\n\t"
        "v_mfma_f32_16x16x32_bf16 v[156:159], v[28:31], v[72:75], v[156:159]\n\t"
        "ds_read_b128 v[56:59], v15 offset:5120\n\t"
        "v_cvt_pk_bf16_f32 v128, v128, v129\n\t"
        "v_mfma_f32_16x16x32_bf16 v[160:163], v[32:35], v[72:75], v[160:163]\n\t"
        "ds_read_b128 v[60:63], v15 offset:6400\n\t"
        "v_cvt_pk_bf16_f32 v129, v130, v131\n\t"
        "v_mfma_f32_16x16x32_bf16 v[164:167], v[36:39], v[72:75], v[164:167]\n\t"
        "ds_read_b64_tr_b16 v[28:29], %[wa0] offset:24576\n\t"
        "ds_write_b64 v12, v[128:129] offset:3072\n\t"
        "s_waitcnt lgkmcnt(8)\n\t"
        "v_mfma_f32_16x16x32_bf16 v[132:135], v[16:19], v[68:71], v[132:135]\n\t"
        "ds_read_b64_tr_b16 v[30:31], %[wa0] offset:25344\n\t"
        "v_cvt_pk_bf16_f32 v108, v108, v109\n\t"
        "v_mfma_f32_16x16x32_bf16 v[136:139], v[20:23], v[68:71], v[136:139]\n\t"
        "ds_read_b64_tr_b16 v[32:33], %[wa1] offset:24576\n\t"
        "v_cvt_pk_bf16_f32 v109, v110, v111\n\t"
        "s_waitcnt lgkmcnt(8)\n\t"
        "v_mfma_f32_16x16x32_bf16 v[140:143], v[24:27], v[68:71], v[140:143]\n\t"
        "ds_read_b64_tr_b16 v[34:35], %[wa1] offset:25344\n\t"
        "v_cvt_pk_bf16_f32 v110, v112, v113\n\t"
        "v_mfma_f32_16x16x32_bf16 v[152:155], v[24:27], v[72:75], v[152:155]\n\t"
        "ds_read_b64_tr_b16 v[36:37], %[wa2] offset:24576\n\t"
        "v_cvt_pk_bf16_f32 v111, v114, v115\n\t"
        "v_mfma_f32_16x16x32_bf16 v[148:151], v[20:23], v[72:75], v[148:151]\n\t"
        "ds_read_b64_tr_b16 v[38:39], %[wa2] offset:25344\n\t"
        "s_mov_b32 s40, 86016\n\t"
        "v_mfma_f32_16x16x32_bf16 v[144:147], v[16:19], v[72:75], v[144:147]\n\t"
        "ds_read_b128 v[64:67], v15 offset:7680\n\t"
        "s_nop 0\n\t"
        "v_mfma_f32_16x16x32_bf16 v[156:159], v[16:19], v[76:79], v[156:159]\n\t"
        "v_permlane16_swap_b32 v108, v110\n\t"
        "v_permlane16_swap_b32 v109, v111\n\t"
        "v_mfma_f32_16x16x32_bf16 v[160:163], v[20:23], v[76:79], v[160:163]\n\t"
        "buffer_store_dwordx4 v[108:111], %[vo], %[ro], s40 offen sc1\n\t"
        "s_waitcnt lgkmcnt(6)\n\t"
        "v_mfma_f32_16x16x32_bf16 v[164:167], v[24:27], v[76:79], v[164:167]\n\t"
        "ds_read_b64_tr_b16 v[16:17], %[wa0] offset:30720\n\t"
        "s_barrier\n\t"
        "s_waitcnt lgkmcnt(4)\n\t"
        "v_mfma_f32_16x16x32_bf16 v[132:135], v[28:31], v[72:75], v[132:135]\n\t"
        "ds_read_b64_tr_b16 v[18:19], %[wa0] offset:31488\n\t"
        "ds_read_b128 v[92:95], v13 offset:0\n\t"
        "v_mfma_f32_16x16x32_bf16 v[136:139], v[32:35], v[72:75], v[136:139]\n\t"
        "ds_read_b64_tr_b16 v[20:21], %[wa1] offset:30720\n\t"
        "ds_read_b128 v[116:119], v13 offset:2048\n\t"
        "s_waitcnt lgkmcnt(6)\n\t"
        "v_mfma_f32_16x16x32_bf16 v[140:143], v[36:39], v[72:75], v[140:143]\n\t"
        "ds_read_b64_tr_b16 v[22:23], %[wa1] offset:31488\n\t"
        "v_cvt_pk_bf16_f32 v120, v120, v121\n\t"
        "v_mfma_f32_16x16x32_bf16 v[152:155], v[36:39], v[76:79], v[152:155]\n\t"
        "ds_read_b64_tr_b16 v[24:25], %[wa2] offset:30720\n\t"
        "v_cvt_pk_bf16_f32 v121, v122, v123\n\t"
        "v_mfma_f32_16x16x32_bf16 v[148:151], v[32:35], v[76:79], v[148:151]\n\t"
        "ds_read_b64_tr_b16 v[26:27], %[wa2] offset:31488\n\t"
        "v_cvt_pk_bf16_f32 v122, v124, v125\n\t"
        "v_mfma_f32_16x16x32_bf16 v[144:147], v[28:31], v[76:79], v[144:147]\n\t"
        "ds_read_b128 v[68:71], v15 offset:8960\n\t"
        "v_cvt_pk_bf16_f32 v123, v126, v127\n\t"
        "v_mfma_f32_16x16x32_bf16 v[156:159], v[28:31], v[80:83], v[156:159]\n\t"
        "s_mov_b32 s40, 129024\n\t"
        "s_nop 0\n\t"
        "v_mfma_f32_16x16x32_bf16 v[160:163], v[32:35], v[80:83], v[160:163]\n\t"
        "v_permlane16_swap_b32 v120, v122\n\t"
        "v_permlane16_swap_b32 v121, v123\n\t"
        "v_mfma_f32_16x16x32_bf16 v[164:167], v[36:39], v[80:83], v[164:167]\n\t"
        "ds_read_b64_tr_b16 v[28:29], %[wa0] offset:36864\n\t"
        "buffer_store_dwordx4 v[120:123], %[vo], %[ro], s40 offen sc1\n\t"
        "s_waitcnt lgkmcnt(4)\n\t"
        "v_mfma_f32_16x16x32_bf16 v[132:135], v[16:19], v[56:59], v[132:135]\n\t"
        "ds_read_b64_tr_b16 v[30:31], %[wa0] offset:37632\n\t"
        "s_mov_b32 s40, 0\n\t"
        "v_mfma_f32_16x16x32_bf16 v[136:139], v[20:23], v[56:59], v[136:139]\n\t"
        "ds_read_b64_tr_b16 v[32:33], %[wa1] offset:36864\n\t"
        "s_waitcnt lgkmcnt(4)\n\t"
        "v_mfma_f32_16x16x32_bf16 v[140:143], v[24:27], v[56:59], v[140:143]\n\t"
        "ds_read_b64_tr_b16 v[34:35], %[wa1] offset:37632\n\t"
        "buffer_store_dwordx4 v[92:95], %[vo2], %[ro], s40 offen sc1\n\t"
        "v_mfma_f32_16x16x32_bf16 v[152:155], v[24:27], v[60:63], v[152:155]\n\t"
        "ds_read_b64_tr_b16 v[36:37], %[wa2] offset:36864\n\t"
        "s_mov_b32 s40, 86016\n\t"
        "v_mfma_f32_16x16x32_bf16 v[148:151], v[20:23], v[60:63], v[148:151]\n\t"
        "ds_read_b64_tr_b16 v[38:39], %[wa2] offset:37632\n\t"
        "v_mfma_f32_16x16x32_bf16 v[144:147], v[16:19], v[60:63], v[144:147]\n\t"
        "ds_read_b128 v[72:75], v15 offset:10240\n\t"
        "buffer_store_dwordx4 v[116:119], %[vo2], %[ro], s40 offen sc1\n\t"
        "v_mfma_f32_16x16x32_bf16 v[156:159], v[16:19], v[64:67], v[156:159]\n\t"
        "v_mfma_f32_16x16x32_bf16 v[160:163], v[20:23], v[64:67], v[160:163]\n\t"
        "v_mfma_f32_16x16x32_bf16 v[164:167], v[24:27], v[64:67], v[164:167]\n\t"
        "ds_read_b64_tr_b16 v[16:17], %[wa0] offset:43008\n\t"
        "s_waitcnt lgkmcnt(4)\n\t"
        "v_mfma_f32_16x16x32_bf16 v[132:135], v[28:31], v[60:63], v[132:135]\n\t"
        "ds_read_b64_tr_b16 v[18:19], %[wa0] offset:43776\n\t"
        "v_mfma_f32_16x16x32_bf16 v[136:139], v[32:35], v[60:63], v[136:139]\n\t"
        "ds_read_b64_tr_b16 v[20:21], %[wa1] offset:43008\n\t"
        "s_waitcnt lgkmcnt(4)\n\t"
        "v_mfma_f32_16x16x32_bf16 v[140:143], v[36:39], v[60:63], v[140:143]\n\t"
        "ds_read_b64_tr_b16 v[22:23], %[wa1] offset:43776\n\t"
        "v_mfma_f32_16x16x32_bf16 v[152:155], v[36:39], v[64:67], v[152:155]\n\t"
        "ds_read_b64_tr_b16 v[24:25], %[wa2] offset:43008\n\t"
        "v_mfma_f32_16x16x32_bf16 v[148:151], v[32:35], v[64:67], v[148:151]\n\t"
        "ds_read_b64_tr_b16 v[26:27], %[wa2] offset:43776\n\t"
        "v_mfma_f32_16x16x32_bf16 v[144:147], v[28:31], v[64:67], v[144:147]\n\t"
        "ds_read_b128 v[76:79], v15 offset:11520\n\t"
        "v_mfma_f32_16x16x32_bf16 v[156:159], v[28:31], v[68:71], v[156:159]\n\t"
        "v_mfma_f32_16x16x32_bf16 v[160:163], v[32:35], v[68:71], v[160:163]\n\t"
        "v_mfma_f32_16x16x32_bf16 v[164:167], v[36:39], v[68:71], v[164:167]\n\t"
        "ds_read_b64_tr_b16 v[28:29], %[wa0] offset:49152\n\t"
        "s_waitcnt lgkmcnt(4)\n\t"
        "v_mfma_f32_16x16x32_bf16 v[132:135], v[16:19], v[64:67], v[132:135]\n\t"
        "ds_read_b64_tr_b16 v[30:31], %[wa0] offset:49920\n\t"
        "v_mfma_f32_16x16x32_bf16 v[136:139], v[20:23], v[64:67], v[136:139]\n\t"
        "ds_read_b64_tr_b16 v[32:33], %[wa1] offset:49152\n\t"
        "s_waitcnt lgkmcnt(4)\n\t"
        "v_mfma_f32_16x16x32_bf16 v[140:143], v[24:27], v[64:67], v[140:143]\n\t"
        "ds_read_b64_tr_b16 v[34:35], %[wa1] offset:49920\n\t"
        "v_mfma_f32_16x16x32_bf16 v[152:155], v[24:27], v[68:71], v[152:155]\n\t"
        "ds_read_b64_tr_b16 v[36:37], %[wa2] offset:49152\n\t"
        "v_mfma_f32_16x16x32_bf16 v[148:151], v[20:23], v[68:71], v[148:151]\n\t"
        "ds_read_b64_tr_b16 v[38:39], %[wa2] offset:49920\n\t"
        "v_mfma_f32_16x16x32_bf16 v[144:147], v[16:19], v[68:71], v[144:147]\n\t"
        "ds_read_b128 v[80:83], v15 offset:12800\n\t"
        "v_mfma_f32_16x16x32_bf16 v[156:159], v[16:19], v[72:75], v[156:159]\n\t"
        "v_mfma_f32_16x16x32_bf16 v[160:163], v[20:23], v[72:75], v[160:163]\n\t"
        "v_mfma_f32_16x16x32_bf16 v[164:167], v[24:27], v[72:75], v[164:167]\n\t"
        "ds_read_b64_tr_b16 v[16:17], %[wa0] offset:55296\n\t"
        "s_waitcnt lgkmcnt(4)\n\t"
        "v_mfma_f32_16x16x32_bf16 v[132:135], v[28:31], v[68:71], v[132:135]\n\t"
        "ds_read_b64_tr_b16 v[18:19], %[wa0] offset:56064\n\t"
        "v_mfma_f32_16x16x32_bf16 v[136:139], v[32:35], v[68:71], v[136:139]\n\t"
        "ds_read_b64_tr_b16 v[20:21], %[wa1] offset:55296\n\t"
        "s_waitcnt lgkmcnt(4)\n\t"
        "v_mfma_f32_16x16x32_bf16 v[140:143], v[36:39], v[68:71], v[140:143]\n\t"
        "ds_read_b64_tr_b16 v[22:23], %[wa1] offset:56064\n\t"
        "v_mfma_f32_16x16x32_bf16 v[152:155], v[36:39], v[72:75], v[152:155]\n\t"
        "ds_read_b64_tr_b16 v[24:25], %[wa2] offset:55296\n\t"
        "v_mfma_f32_16x16x32_bf16 v[148:151], v[32:35], v[72:75], v[148:151]\n\t"
        "ds_read_b64_tr_b16 v[26:27], %[wa2] offset:56064\n\t"
        "v_mfma_f32_16x16x32_bf16 v[144:147], v[28:31], v[72:75], v[144:147]\n\t"
        "v_mfma_f32_16x16x32_bf16 v[156:159], v[28:31], v[76:79], v[156:159]\n\t"
        "v_mfma_f32_16x16x32_bf16 v[160:163], v[32:35], v[76:79], v[160:163]\n\t"
        "v_mfma_f32_16x16x32_bf16 v[164:167], v[36:39], v[76:79], v[164:167]\n\t"
        "s_waitcnt lgkmcnt(2)\n\t"
        "v_mfma_f32_16x16x32_bf16 v[132:135], v[16:19], v[72:75], v[132:135]\n\t"
        "v_mfma_f32_16x16x32_bf16 v[136:139], v[20:23], v[72:75], v[136:139]\n\t"
        "s_waitcnt lgkmcnt(0)\n\t"
        "v_mfma_f32_16x16x32_bf16 v[140:143], v[24:27], v[72:75], v[140:143]\n\t"
        "v_mfma_f32_16x16x32_bf16 v[152:155], v[24:27], v[76:79], v[152:155]\n\t"
        "v_mfma_f32_16x16x32_bf16 v[148:151], v[20:23], v[76:79], v[148:151]\n\t"
        "v_mfma_f32_16x16x32_bf16 v[144:147], v[16:19], v[76:79], v[144:147]\n\t"
        "v_mfma_f32_16x16x32_bf16 v[156:159], v[16:19], v[80:83], v[156:159]\n\t"
        "v_mfma_f32_16x16x32_bf16 v[160:163], v[20:23], v[80:83], v[160:163]\n\t"
        "v_mfma_f32_16x16x32_bf16 v[164:167], v[24:27], v[80:83], v[164:167]\n\t"
        "s_nop 7\n\t"
        "v_cvt_pk_bf16_f32 v140, v140, v141\n\t"
        "v_cvt_pk_bf16_f32 v141, v142, v143\n\t"
        "ds_write_b64 v12, v[140:141] offset:4096\n\t"
        "v_cvt_pk_bf16_f32 v152, v152, v153\n\t"
        "v_cvt_pk_bf16_f32 v153, v154, v155\n\t"
        "ds_write_b64 v12, v[152:153] offset:5120\n\t"
        "v_cvt_pk_bf16_f32 v164, v164, v165\n\t"
        "v_cvt_pk_bf16_f32 v165, v166, v167\n\t"
        "ds_write_b64 v12, v[164:165] offset:6144\n\t"
        "v_cvt_pk_bf16_f32 v132, v132, v133\n\t"
        "v_cvt_pk_bf16_f32 v133, v134, v135\n\t"
        "v_cvt_pk_bf16_f32 v134, v136, v137\n\t"
        "v_cvt_pk_bf16_f32 v135, v138, v139\n\t"
        "s_mov_b32 s40, 172032\n\t"
        "s_nop 0\n\t"
        "v_permlane16_swap_b32 v132, v134\n\t"
        "v_permlane16_swap_b32 v133, v135\n\t"
        "buffer_store_dwordx4 v[132:135], %[vo], %[ro], s40 offen sc1\n\t"
        "s_waitcnt lgkmcnt(0)\n\t"
        "s_barrier\n\t"
        "ds_read_b128 v[140:143], v13 offset:4096\n\t"
        "ds_read_b128 v[164:167], v13 offset:6144\n\t"
        "v_cvt_pk_bf16_f32 v144, v144, v145\n\t"
        "v_cvt_pk_bf16_f32 v145, v146, v147\n\t"
        "v_cvt_pk_bf16_f32 v146, v148, v149\n\t"
        "v_cvt_pk_bf16_f32 v147, v150, v151\n\t"
        "s_mov_b32 s40, 215040\n\t"
        "s_nop 0\n\t"
        "v_permlane16_swap_b32 v144, v146\n\t"
        "v_permlane16_swap_b32 v145, v147\n\t"
        "buffer_store_dwordx4 v[144:147], %[vo], %[ro], s40 offen sc1\n\t"
        "v_cvt_pk_bf16_f32 v156, v156, v157\n\t"
        "v_cvt_pk_bf16_f32 v157, v158, v159\n\t"
        "v_cvt_pk_bf16_f32 v158, v160, v161\n\t"
        "v_cvt_pk_bf16_f32 v159, v162, v163\n\t"
        "s_mov_b32 s40, 258048\n\t"
        "s_nop 0\n\t"
        "v_permlane16_swap_b32 v156, v158\n\t"
        "v_permlane16_swap_b32 v157, v159\n\t"
        "buffer_store_dwordx4 v[156:159], %[vo], %[ro], s40 offen sc1\n\t"
        "s_mov_b32 s40, 172032\n\t"
        "s_waitcnt lgkmcnt(1)\n\t"
        "buffer_store_dwordx4 v[140:143], %[vo2], %[ro], s40 offen sc1\n\t"
        "s_cmp_lg_u32 %[nh], 0\n\t"
        "s_cbranch_scc1 8f\n\t"
        "s_mov_b32 s40, 258048\n\t"
        "s_waitcnt lgkmcnt(0)\n\t"
        "buffer_store_dwordx4 v[164:167], %[vo2], %[ro], s40 offen sc1\n\t"
        "s_branch 8f\n\t"
        "2:\n\t"
        "s_nop 7\n\t"
        "s_setprio 0\n\t"
        "s_waitcnt vmcnt(0) lgkmcnt(0)\n\t"
        "s_barrier\n\t"
        "v_mbcnt_lo_u32_b32 v17, -1, 0\n\t"
        "v_mbcnt_hi_u32_b32 v17, -1, v17\n\t"
        "v_lshl_add_u32 v17, %[wv], 6, v17\n\t"
        "s_mov_b64 s[60:61], %[ka]\n\t"
        "s_or_b32 s62, %[bid], 0x100\n\t"
        "s_setpc_b64 %[re]\n\t"
        "1:\n\t"
        "ds_read_b128 v[16:19], %[ta]\n\t"
        "ds_read_b128 v[20:23], %[ta] offset:256\n\t"
        "s_cmp_lg_u32 %[cv], 4\n\t"
        "s_cbranch_scc1 4f\n\t"
        "ds_read_b32 v22, %[ta] offset:776\n\t"
        "4:\n\t"
        "ds_read_b32 v13, %[ca]\n\t"
        "ds_read_b64_tr_b16 v[32:33], %[wa0] offset:12288\n\t"
        "ds_read_b64_tr_b16 v[34:35], %[wa0] offset:13056\n\t"
        "ds_read_b64_tr_b16 v[36:37], %[wa1] offset:12288\n\t"
        "ds_read_b64_tr_b16 v[38:39], %[wa1] offset:13056\n\t"
        "ds_read_b64_tr_b16 v[40:41], %[wa2] offset:12288\n\t"
        "ds_read_b64_tr_b16 v[42:43], %[wa2] offset:13056\n\t"
        "v_mov_b64 v[84:85], 0\n\t"
        "v_mov_b64 v[86:87], 0\n\t"
        "v_mov_b64 v[88:89], 0\n\t"
        "v_mov_b64 v[90:91], 0\n\t"
        "v_mov_b64 v[92:93], 0\n\t"
        "v_mov_b64 v[94:95], 0\n\t"
        "v_mov_b64 v[96:97], 0\n\t"
        "v_mov_b64 v[98:99], 0\n\t"
        "v_mov_b64 v[100:101], 0\n\t"
        "v_mov_b64 v[102:103], 0\n\t"
        "v_mov_b64 v[104:105], 0\n\t"
        "v_mov_b64 v[106:107], 0\n\t"
        "v_mov_b64 v[108:109], 0\n\t"
        "v_mov_b64 v[110:111], 0\n\t"
        "v_mov_b64 v[112:113], 0\n\t"
        "v_mov_b64 v[114:115], 0\n\t"
        "v_mov_b64 v[116:117], 0\n\t"
        "v_mov_b64 v[118:119], 0\n\t"
        "v_mov_b64 v[120:121], 0\n\t"
        "v_mov_b64 v[122:123], 0\n\t"
        "v_mov_b64 v[124:125], 0\n\t"
        "v_mov_b64 v[126:127], 0\n\t"
        "v_mov_b64 v[128:129], 0\n\t"
        "v_mov_b64 v[130:131], 0\n\t"
        "v_mov_b64 v[132:133], 0\n\t"
        "v_mov_b64 v[134:135], 0\n\t"
        "v_mov_b64 v[136:137], 0\n\t"
        "v_mov_b64 v[138:139], 0\n\t"
        "v_mov_b64 v[140:141], 0\n\t"
        "v_mov_b64 v[142:143], 0\n\t"
        "v_mov_b64 v[144:145], 0\n\t"
        "v_mov_b64 v[146:147], 0\n\t"
        "v_mov_b64 v[148:149], 0\n\t"
        "v_mov_b64 v[150:151], 0\n\t"
        "v_mov_b64 v[152:153], 0\n\t"
        "v_mov_b64 v[154:155], 0\n\t"
        "v_mov_b64 v[156:157], 0\n\t"
        "v_mov_b64 v[158:159], 0\n\t"
        "v_mov_b64 v[160:161], 0\n\t"
        "v_mov_b64 v[162:163], 0\n\t"
        "v_mov_b64 v[164:165], 0\n\t"
        "v_mov_b64 v[166:167], 0\n\t"
        "s_waitcnt lgkmcnt(6)\n\t"
        "v_cmp_lt_u16 vcc, 0, v13\n\t"
        "v_xor_b32 v24, %[q16], v16\n\t"
        "v_xor_b32 v25, %[q16], v17\n\t"
        "v_xor_b32 v26, %[q16], v18\n\t"
        "v_xor_b32 v27, %[q16], v19\n\t"
        "v_xor_b32 v28, %[q16], v20\n\t"
        "v_xor_b32 v29, %[q16], v21\n\t"
        "v_xor_b32 v30, %[q16], v22\n\t"
        "s_waitcnt vmcnt(3) lgkmcnt(0)\n\t"
        "s_barrier\n\t"
        "s_cbranch_vccz 3f\n\t"
        "s_barrier\n\t"
        "3:\n\t"
        "ds_read_b128 v[56:59], v24 offset:0\n\t"
        "ds_read_b128 v[60:63], v25 offset:0\n\t"
        "ds_read_b128 v[64:67], v26 offset:0\n\t"
        "ds_read_b128 v[68:71], v27 offset:0\n\t"
        "ds_read_b128 v[72:75], v28 offset:0\n\t"
        "ds_read_b128 v[76:79], v29 offset:0\n\t"
        "s_cmp_eq_u32 %[cv], 6\n\t"
        "s_cbranch_scc1 65f\n\t"
        "ds_read_b128 v[80:83], v30 offset:0\n\t"
        "s_cmp_eq_u32 %[cv], 5\n\t"
        "s_cbranch_scc1 55f\n\t"
        "s_cmp_eq_u32 %[cv], 4\n\t"
        "s_cbranch_scc1 45f\n\t"
        "75:\n\t"
        "s_setprio 2\n\t"
        "s_waitcnt lgkmcnt(0)\n\t"
        ".p2align 6\n\t"
        "70:\n\t"
        "v_mfma_f32_16x16x32_bf16 v[84:87], v[32:35], v[56:59], v[84:87]\n\t"
        "ds_read_b64_tr_b16 v[44:45], %[wa0] offset:18432\n\t"
        "v_mfma_f32_16x16x32_bf16 v[88:91], v[36:39], v[56:59], v[88:91]\n\t"
        "ds_read_b64_tr_b16 v[46:47], %[wa0] offset:19200\n\t"
        "v_mfma_f32_16x16x32_bf16 v[92:95], v[40:43], v[56:59], v[92:95]\n\t"
        "ds_read_b128 v[56:59], v24 offset:64\n\t"
        "s_waitcnt lgkmcnt(8)\n\t"
        "v_mfma_f32_16x16x32_bf16 v[104:107], v[40:43], v[60:63], v[104:107]\n\t"
        "ds_read_b64_tr_b16 v[48:49], %[wa1] offset:18432\n\t"
        "v_mfma_f32_16x16x32_bf16 v[100:103], v[36:39], v[60:63], v[100:103]\n\t"
        "ds_read_b64_tr_b16 v[50:51], %[wa1] offset:19200\n\t"
        "v_mfma_f32_16x16x32_bf16 v[96:99], v[32:35], v[60:63], v[96:99]\n\t"
        "ds_read_b128 v[60:63], v25 offset:64\n\t"
        "s_waitcnt lgkmcnt(10)\n\t"
        "v_mfma_f32_16x16x32_bf16 v[108:111], v[32:35], v[64:67], v[108:111]\n\t"
        "ds_read_b64_tr_b16 v[52:53], %[wa2] offset:18432\n\t"
        "v_mfma_f32_16x16x32_bf16 v[112:115], v[36:39], v[64:67], v[112:115]\n\t"
        "ds_read_b64_tr_b16 v[54:55], %[wa2] offset:19200\n\t"
        "v_mfma_f32_16x16x32_bf16 v[116:119], v[40:43], v[64:67], v[116:119]\n\t"
        "ds_read_b128 v[64:67], v26 offset:64\n\t"
        "s_waitcnt lgkmcnt(12)\n\t"
        "v_mfma_f32_16x16x32_bf16 v[128:131], v[40:43], v[68:71], v[128:131]\n\t"
        "ds_read2_b32 v[14:15], %[ca] offset0:0 offset1:1\n\t"
        "v_mfma_f32_16x16x32_bf16 v[124:127], v[36:39], v[68:71], v[124:127]\n\t"
        "ds_read_b128 v[16:19], %[ta] offset:1024\n\t"
        "v_mfma_f32_16x16x32_bf16 v[120:123], v[32:35], v[68:71], v[120:123]\n\t"
        "ds_read_b128 v[68:71], v27 offset:64\n\t"
        "s_waitcnt lgkmcnt(14)\n\t"
        "v_mfma_f32_16x16x32_bf16 v[132:135], v[32:35], v[72:75], v[132:135]\n\t"
        "ds_read_b128 v[20:23], %[ta] offset:1280\n\t"
        "v_mfma_f32_16x16x32_bf16 v[136:139], v[36:39], v[72:75], v[136:139]\n\t"
        "v_mfma_f32_16x16x32_bf16 v[140:143], v[40:43], v[72:75], v[140:143]\n\t"
        "ds_read_b128 v[72:75], v28 offset:64\n\t"
        "s_waitcnt lgkmcnt(14)\n\t"
        "v_mfma_f32_16x16x32_bf16 v[152:155], v[40:43], v[76:79], v[152:155]\n\t"
        "v_mfma_f32_16x16x32_bf16 v[148:151], v[36:39], v[76:79], v[148:151]\n\t"
        "v_mfma_f32_16x16x32_bf16 v[144:147], v[32:35], v[76:79], v[144:147]\n\t"
        "ds_read_b128 v[76:79], v29 offset:64\n\t"
        "v_mfma_f32_16x16x32_bf16 v[156:159], v[32:35], v[80:83], v[156:159]\n\t"
        "v_mfma_f32_16x16x32_bf16 v[160:163], v[36:39], v[80:83], v[160:163]\n\t"
        "s_waitcnt lgkmcnt(2)\n\t"
        "v_add_u32 v14, v14, v15\n\t"
        "v_mfma_f32_16x16x32_bf16 v[164:167], v[40:43], v[80:83], v[164:167]\n\t"
        "ds_read_b128 v[80:83], v30 offset:64\n\t"
        "v_cmp_lt_u16 vcc, 208, v14\n\t"
        "v_mfma_f32_16x16x32_bf16 v[84:87], v[44:47], v[56:59], v[84:87]\n\t"
        "ds_read_b64_tr_b16 v[32:33], %[wa0] offset:24576\n\t"
        "v_mfma_f32_16x16x32_bf16 v[88:91], v[48:51], v[56:59], v[88:91]\n\t"
        "ds_read_b64_tr_b16 v[34:35], %[wa0] offset:25344\n\t"
        "v_mfma_f32_16x16x32_bf16 v[92:95], v[52:55], v[56:59], v[92:95]\n\t"
        "ds_read_b128 v[56:59], v24 offset:128\n\t"
        "v_mfma_f32_16x16x32_bf16 v[104:107], v[52:55], v[60:63], v[104:107]\n\t"
        "ds_read_b64_tr_b16 v[36:37], %[wa1] offset:24576\n\t"
        "v_xor_b32 v24, %[q16], v16\n\t"
        "v_mfma_f32_16x16x32_bf16 v[100:103], v[48:51], v[60:63], v[100:103]\n\t"
        "ds_read_b64_tr_b16 v[38:39], %[wa1] offset:25344\n\t"
        "v_mfma_f32_16x16x32_bf16 v[96:99], v[44:47], v[60:63], v[96:99]\n\t"
        "ds_read_b128 v[60:63], v25 offset:128\n\t"
        "v_mfma_f32_16x16x32_bf16 v[108:111], v[44:47], v[64:67], v[108:111]\n\t"
        "ds_read_b64_tr_b16 v[40:41], %[wa2] offset:24576\n\t"
        "v_xor_b32 v25, %[q16], v17\n\t"
        "v_mfma_f32_16x16x32_bf16 v[112:115], v[48:51], v[64:67], v[112:115]\n\t"
        "ds_read_b64_tr_b16 v[42:43], %[wa2] offset:25344\n\t"
        "v_mfma_f32_16x16x32_bf16 v[116:119], v[52:55], v[64:67], v[116:119]\n\t"
        "ds_read_b128 v[64:67], v26 offset:128\n\t"
        "v_mfma_f32_16x16x32_bf16 v[128:131], v[52:55], v[68:71], v[128:131]\n\t"
        "v_xor_b32 v26, %[q16], v18\n\t"
        "v_mfma_f32_16x16x32_bf16 v[124:127], v[48:51], v[68:71], v[124:127]\n\t"
        "s_waitcnt lgkmcnt(11)\n\t"
        "v_mfma_f32_16x16x32_bf16 v[120:123], v[44:47], v[68:71], v[120:123]\n\t"
        "ds_read_b128 v[68:71], v27 offset:128\n\t"
        "v_mfma_f32_16x16x32_bf16 v[132:135], v[44:47], v[72:75], v[132:135]\n\t"
        "v_xor_b32 v27, %[q16], v19\n\t"
        "v_mfma_f32_16x16x32_bf16 v[136:139], v[48:51], v[72:75], v[136:139]\n\t"
        "s_waitcnt lgkmcnt(11)\n\t"
        "v_mfma_f32_16x16x32_bf16 v[140:143], v[52:55], v[72:75], v[140:143]\n\t"
        "ds_read_b128 v[72:75], v28 offset:128\n\t"
        "v_mfma_f32_16x16x32_bf16 v[152:155], v[52:55], v[76:79], v[152:155]\n\t"
        "v_xor_b32 v28, %[q16], v20\n\t"
        "v_mfma_f32_16x16x32_bf16 v[148:151], v[48:51], v[76:79], v[148:151]\n\t"
        "s_waitcnt lgkmcnt(11)\n\t"
        "v_mfma_f32_16x16x32_bf16 v[144:147], v[44:47], v[76:79], v[144:147]\n\t"
        "ds_read_b128 v[76:79], v29 offset:128\n\t"
        "v_mfma_f32_16x16x32_bf16 v[156:159], v[44:47], v[80:83], v[156:159]\n\t"
        "v_xor_b32 v29, %[q16], v21\n\t"
        "v_mfma_f32_16x16x32_bf16 v[160:163], v[48:51], v[80:83], v[160:163]\n\t"
        "s_waitcnt lgkmcnt(4)\n\t"
        "v_mfma_f32_16x16x32_bf16 v[164:167], v[52:55], v[80:83], v[164:167]\n\t"
        "ds_read_b128 v[80:83], v30 offset:128\n\t"
        "v_mfma_f32_16x16x32_bf16 v[84:87], v[32:35], v[56:59], v[84:87]\n\t"
        "ds_read_b64_tr_b16 v[44:45], %[wa0] offset:30720\n\t"
        "v_xor_b32 v30, %[q16], v22\n\t"
        "v_mfma_f32_16x16x32_bf16 v[88:91], v[36:39], v[56:59], v[88:91]\n\t"
        "ds_read_b64_tr_b16 v[46:47], %[wa0] offset:31488\n\t"
        "v_mfma_f32_16x16x32_bf16 v[92:95], v[40:43], v[56:59], v[92:95]\n\t"
        "ds_read_b64_tr_b16 v[48:49], %[wa1] offset:30720\n\t"
        "v_mfma_f32_16x16x32_bf16 v[104:107], v[40:43], v[60:63], v[104:107]\n\t"
        "ds_read_b64_tr_b16 v[50:51], %[wa1] offset:31488\n\t"
        "v_mfma_f32_16x16x32_bf16 v[100:103], v[36:39], v[60:63], v[100:103]\n\t"
        "ds_read_b64_tr_b16 v[52:53], %[wa2] offset:30720\n\t"
        "s_waitcnt lgkmcnt(9)\n\t"
        "v_mfma_f32_16x16x32_bf16 v[96:99], v[32:35], v[60:63], v[96:99]\n\t"
        "ds_read_b64_tr_b16 v[54:55], %[wa2] offset:31488\n\t"
        "v_mfma_f32_16x16x32_bf16 v[108:111], v[32:35], v[64:67], v[108:111]\n\t"
        "v_mfma_f32_16x16x32_bf16 v[112:115], v[36:39], v[64:67], v[112:115]\n\t"
        "s_waitcnt lgkmcnt(9)\n\t"
        "v_mfma_f32_16x16x32_bf16 v[116:119], v[40:43], v[64:67], v[116:119]\n\t"
        "s_waitcnt lgkmcnt(0)\n\t"
        "s_barrier\n\t"
        "s_cbranch_vccnz 701f\n\t"
        "702:\n\t"
        "v_mfma_f32_16x16x32_bf16 v[128:131], v[40:43], v[68:71], v[128:131]\n\t"
        "ds_read_b128 v[56:59], v24 offset:0\n\t"
        "v_mfma_f32_16x16x32_bf16 v[124:127], v[36:39], v[68:71], v[124:127]\n\t"
        "ds_read_b128 v[60:63], v25 offset:0\n\t"
        "v_mfma_f32_16x16x32_bf16 v[120:123], v[32:35], v[68:71], v[120:123]\n\t"
        "ds_read_b128 v[64:67], v26 offset:0\n\t"
        "v_mfma_f32_16x16x32_bf16 v[132:135], v[32:35], v[72:75], v[132:135]\n\t"
        "ds_read_b128 v[68:71], v27 offset:0\n\t"
        "v_mfma_f32_16x16x32_bf16 v[136:139], v[36:39], v[72:75], v[136:139]\n\t"
        "v_mfma_f32_16x16x32_bf16 v[140:143], v[40:43], v[72:75], v[140:143]\n\t"
        "ds_read_b128 v[72:75], v28 offset:0\n\t"
        "v_mfma_f32_16x16x32_bf16 v[152:155], v[40:43], v[76:79], v[152:155]\n\t"
        "v_mfma_f32_16x16x32_bf16 v[148:151], v[36:39], v[76:79], v[148:151]\n\t"
        "v_mfma_f32_16x16x32_bf16 v[144:147], v[32:35], v[76:79], v[144:147]\n\t"
        "ds_read_b128 v[76:79], v29 offset:0\n\t"
        "v_mfma_f32_16x16x32_bf16 v[156:159], v[32:35], v[80:83], v[156:159]\n\t"
        "v_mfma_f32_16x16x32_bf16 v[160:163], v[36:39], v[80:83], v[160:163]\n\t"
        "s_sub_u32 %[tn], %[tn], 1\n\t"
        "v_mfma_f32_16x16x32_bf16 v[164:167], v[40:43], v[80:83], v[164:167]\n\t"
        "ds_read_b128 v[80:83], v30 offset:0\n\t"
        "s_cbranch_scc1 9f\n\t"
        "s_waitcnt lgkmcnt(6)\n\t"
        "v_mfma_f32_16x16x32_bf16 v[84:87], v[44:47], v[56:59], v[84:87]\n\t"
        "ds_read_b64_tr_b16 v[32:33], %[wa0] offset:0\n\t"
        "v_mfma_f32_16x16x32_bf16 v[88:91], v[48:51], v[56:59], v[88:91]\n\t"
        "ds_read_b64_tr_b16 v[34:35], %[wa0] offset:768\n\t"
        "v_mfma_f32_16x16x32_bf16 v[92:95], v[52:55], v[56:59], v[92:95]\n\t"
        "ds_read_b128 v[56:59], v24 offset:64\n\t"
        "s_waitcnt lgkmcnt(8)\n\t"
        "v_mfma_f32_16x16x32_bf16 v[104:107], v[52:55], v[60:63], v[104:107]\n\t"
        "ds_read_b64_tr_b16 v[36:37], %[wa1] offset:0\n\t"
        "v_mfma_f32_16x16x32_bf16 v[100:103], v[48:51], v[60:63], v[100:103]\n\t"
        "ds_read_b64_tr_b16 v[38:39], %[wa1] offset:768\n\t"
        "v_mfma_f32_16x16x32_bf16 v[96:99], v[44:47], v[60:63], v[96:99]\n\t"
        "ds_read_b128 v[60:63], v25 offset:64\n\t"
        "s_waitcnt lgkmcnt(10)\n\t"
        "v_mfma_f32_16x16x32_bf16 v[108:111], v[44:47], v[64:67], v[108:111]\n\t"
        "ds_read_b64_tr_b16 v[40:41], %[wa2] offset:0\n\t"
        "v_mfma_f32_16x16x32_bf16 v[112:115], v[48:51], v[64:67], v[112:115]\n\t"
        "ds_read_b64_tr_b16 v[42:43], %[wa2] offset:768\n\t"
        "v_mfma_f32_16x16x32_bf16 v[116:119], v[52:55], v[64:67], v[116:119]\n\t"
        "ds_read_b128 v[64:67], v26 offset:64\n\t"
        "s_waitcnt lgkmcnt(12)\n\t"
        "v_mfma_f32_16x16x32_bf16 v[128:131], v[52:55], v[68:71], v[128:131]\n\t"
        "ds_read2_b32 v[14:15], %[ca] offset0:1 offset1:2\n\t"
        "v_mfma_f32_16x16x32_bf16 v[124:127], v[48:51], v[68:71], v[124:127]\n\t"
        "ds_read_b128 v[16:19], %[ta] offset:0\n\t"
        "v_mfma_f32_16x16x32_bf16 v[120:123], v[44:47], v[68:71], v[120:123]\n\t"
        "ds_read_b128 v[68:71], v27 offset:64\n\t"
        "s_waitcnt lgkmcnt(14)\n\t"
        "v_mfma_f32_16x16x32_bf16 v[132:135], v[44:47], v[72:75], v[132:135]\n\t"
        "ds_read_b128 v[20:23], %[ta] offset:256\n\t"
        "v_mfma_f32_16x16x32_bf16 v[136:139], v[48:51], v[72:75], v[136:139]\n\t"
        "v_mfma_f32_16x16x32_bf16 v[140:143], v[52:55], v[72:75], v[140:143]\n\t"
        "ds_read_b128 v[72:75], v28 offset:64\n\t"
        "s_waitcnt lgkmcnt(14)\n\t"
        "v_mfma_f32_16x16x32_bf16 v[152:155], v[52:55], v[76:79], v[152:155]\n\t"
        "v_mfma_f32_16x16x32_bf16 v[148:151], v[48:51], v[76:79], v[148:151]\n\t"
        "v_mfma_f32_16x16x32_bf16 v[144:147], v[44:47], v[76:79], v[144:147]\n\t"
        "ds_read_b128 v[76:79], v29 offset:64\n\t"
        "v_mfma_f32_16x16x32_bf16 v[156:159], v[44:47], v[80:83], v[156:159]\n\t"
        "s_cmp_eq_u32 %[tn], 0\n\t"
        "v_mfma_f32_16x16x32_bf16 v[160:163], v[48:51], v[80:83], v[160:163]\n\t"
        "s_waitcnt lgkmcnt(2)\n\t"
        "v_add_u32 v14, v14, v15\n\t"
        "v_mfma_f32_16x16x32_bf16 v[164:167], v[52:55], v[80:83], v[164:167]\n\t"
        "ds_read_b128 v[80:83], v30 offset:64\n\t"
        "v_cmp_lt_u16 vcc, 208, v14\n\t"
        "s_cbranch_scc1 71f\n\t"
        "72:\n\t"
        "v_mfma_f32_16x16x32_bf16 v[84:87], v[32:35], v[56:59], v[84:87]\n\t"
        "ds_read_b64_tr_b16 v[44:45], %[wa0] offset:6144\n\t"
        "v_add_u32 %[ca], 8, %[ca]\n\t"
        "v_mfma_f32_16x16x32_bf16 v[88:91], v[36:39], v[56:59], v[88:91]\n\t"
        "ds_read_b64_tr_b16 v[46:47], %[wa0] offset:6912\n\t"
        "v_mfma_f32_16x16x32_bf16 v[92:95], v[40:43], v[56:59], v[92:95]\n\t"
        "ds_read_b128 v[56:59], v24 offset:128\n\t"
        "v_mfma_f32_16x16x32_bf16 v[104:107], v[40:43], v[60:63], v[104:107]\n\t"
        "ds_read_b64_tr_b16 v[48:49], %[wa1] offset:6144\n\t"
        "v_xor_b32 v24, %[q16], v16\n\t"
        "v_mfma_f32_16x16x32_bf16 v[100:103], v[36:39], v[60:63], v[100:103]\n\t"
        "ds_read_b64_tr_b16 v[50:51], %[wa1] offset:6912\n\t"
        "v_mfma_f32_16x16x32_bf16 v[96:99], v[32:35], v[60:63], v[96:99]\n\t"
        "ds_read_b128 v[60:63], v25 offset:128\n\t"
        "v_mfma_f32_16x16x32_bf16 v[108:111], v[32:35], v[64:67], v[108:111]\n\t"
        "ds_read_b64_tr_b16 v[52:53], %[wa2] offset:6144\n\t"
        "v_xor_b32 v25, %[q16], v17\n\t"
        "v_mfma_f32_16x16x32_bf16 v[112:115], v[36:39], v[64:67], v[112:115]\n\t"
        "ds_read_b64_tr_b16 v[54:55], %[wa2] offset:6912\n\t"
        "v_mfma_f32_16x16x32_bf16 v[116:119], v[40:43], v[64:67], v[116:119]\n\t"
        "ds_read_b128 v[64:67], v26 offset:128\n\t"
        "v_mfma_f32_16x16x32_bf16 v[128:131], v[40:43], v[68:71], v[128:131]\n\t"
        "v_xor_b32 v26, %[q16], v18\n\t"
        "v_mfma_f32_16x16x32_bf16 v[124:127], v[36:39], v[68:71], v[124:127]\n\t"
        "s_waitcnt lgkmcnt(11)\n\t"
        "v_mfma_f32_16x16x32_bf16 v[120:123], v[32:35], v[68:71], v[120:123]\n\t"
        "ds_read_b128 v[68:71], v27 offset:128\n\t"
        "v_mfma_f32_16x16x32_bf16 v[132:135], v[32:35], v[72:75], v[132:135]\n\t"
        "v_xor_b32 v27, %[q16], v19\n\t"
        "v_mfma_f32_16x16x32_bf16 v[136:139], v[36:39], v[72:75], v[136:139]\n\t"
        "s_waitcnt lgkmcnt(11)\n\t"
        "v_mfma_f32_16x16x32_bf16 v[140:143], v[40:43], v[72:75], v[140:143]\n\t"
        "ds_read_b128 v[72:75], v28 offset:128\n\t"
        "v_mfma_f32_16x16x32_bf16 v[152:155], v[40:43], v[76:79], v[152:155]\n\t"
        "v_xor_b32 v28, %[q16], v20\n\t"
        "v_mfma_f32_16x16x32_bf16 v[148:151], v[36:39], v[76:79], v[148:151]\n\t"
        "s_waitcnt lgkmcnt(11)\n\t"
        "v_mfma_f32_16x16x32_bf16 v[144:147], v[32:35], v[76:79], v[144:147]\n\t"
        "ds_read_b128 v[76:79], v29 offset:128\n\t"
        "v_mfma_f32_16x16x32_bf16 v[156:159], v[32:35], v[80:83], v[156:159]\n\t"
        "v_xor_b32 v29, %[q16], v21\n\t"
        "v_mfma_f32_16x16x32_bf16 v[160:163], v[36:39], v[80:83], v[160:163]\n\t"
        "s_waitcnt lgkmcnt(4)\n\t"
        "v_mfma_f32_16x16x32_bf16 v[164:167], v[40:43], v[80:83], v[164:167]\n\t"
        "ds_read_b128 v[80:83], v30 offset:128\n\t"
        "v_mfma_f32_16x16x32_bf16 v[84:87], v[44:47], v[56:59], v[84:87]\n\t"
        "ds_read_b64_tr_b16 v[32:33], %[wa0] offset:12288\n\t"
        "v_xor_b32 v30, %[q16], v22\n\t"
        "v_mfma_f32_16x16x32_bf16 v[88:91], v[48:51], v[56:59], v[88:91]\n\t"
        "ds_read_b64_tr_b16 v[34:35], %[wa0] offset:13056\n\t"
        "v_mfma_f32_16x16x32_bf16 v[92:95], v[52:55], v[56:59], v[92:95]\n\t"
        "ds_read_b64_tr_b16 v[36:37], %[wa1] offset:12288\n\t"
        "v_mfma_f32_16x16x32_bf16 v[104:107], v[52:55], v[60:63], v[104:107]\n\t"
        "ds_read_b64_tr_b16 v[38:39], %[wa1] offset:13056\n\t"
        "v_mfma_f32_16x16x32_bf16 v[100:103], v[48:51], v[60:63], v[100:103]\n\t"
        "ds_read_b64_tr_b16 v[40:41], %[wa2] offset:12288\n\t"
        "s_waitcnt lgkmcnt(9)\n\t"
        "v_mfma_f32_16x16x32_bf16 v[96:99], v[44:47], v[60:63], v[96:99]\n\t"
        "ds_read_b64_tr_b16 v[42:43], %[wa2] offset:13056\n\t"
        "v_mfma_f32_16x16x32_bf16 v[108:111], v[44:47], v[64:67], v[108:111]\n\t"
        "v_mfma_f32_16x16x32_bf16 v[112:115], v[48:51], v[64:67], v[112:115]\n\t"
        "s_waitcnt lgkmcnt(9)\n\t"
        "v_mfma_f32_16x16x32_bf16 v[116:119], v[52:55], v[64:67], v[116:119]\n\t"
        "s_waitcnt vmcnt(0) lgkmcnt(0)\n\t"
        "s_barrier\n\t"
        "s_cbranch_vccnz 711f\n\t"
        "712:\n\t"
        "v_mfma_f32_16x16x32_bf16 v[128:131], v[52:55], v[68:71], v[128:131]\n\t"
        "ds_read_b128 v[56:59], v24 offset:0\n\t"
        "v_mfma_f32_16x16x32_bf16 v[124:127], v[48:51], v[68:71], v[124:127]\n\t"
        "ds_read_b128 v[60:63], v25 offset:0\n\t"
        "v_mfma_f32_16x16x32_bf16 v[120:123], v[44:47], v[68:71], v[120:123]\n\t"
        "ds_read_b128 v[64:67], v26 offset:0\n\t"
        "v_mfma_f32_16x16x32_bf16 v[132:135], v[44:47], v[72:75], v[132:135]\n\t"
        "ds_read_b128 v[68:71], v27 offset:0\n\t"
        "v_mfma_f32_16x16x32_bf16 v[136:139], v[48:51], v[72:75], v[136:139]\n\t"
        "v_mfma_f32_16x16x32_bf16 v[140:143], v[52:55], v[72:75], v[140:143]\n\t"
        "ds_read_b128 v[72:75], v28 offset:0\n\t"
        "v_mfma_f32_16x16x32_bf16 v[152:155], v[52:55], v[76:79], v[152:155]\n\t"
        "v_mfma_f32_16x16x32_bf16 v[148:151], v[48:51], v[76:79], v[148:151]\n\t"
        "v_mfma_f32_16x16x32_bf16 v[144:147], v[44:47], v[76:79], v[144:147]\n\t"
        "ds_read_b128 v[76:79], v29 offset:0\n\t"
        "v_mfma_f32_16x16x32_bf16 v[156:159], v[44:47], v[80:83], v[156:159]\n\t"
        "v_mfma_f32_16x16x32_bf16 v[160:163], v[48:51], v[80:83], v[160:163]\n\t"
        "v_mfma_f32_16x16x32_bf16 v[164:167], v[52:55], v[80:83], v[164:167]\n\t"
        "ds_read_b128 v[80:83], v30 offset:0\n\t"
        "s_waitcnt lgkmcnt(6)\n\t"
        "s_branch 70b\n\t"
        "701:\n\t"
        "s_barrier\n\t"
        "s_branch 702b\n\t"
        "711:\n\t"
        "s_barrier\n\t"
        "s_branch 712b\n\t"
        "71:\n\t"
        "s_cbranch_vccnz 72b\n\t"
        "v_mfma_f32_16x16x32_bf16 v[84:87], v[32:35], v[56:59], v[84:87]\n\t"
        "ds_read_b64_tr_b16 v[44:45], %[wa0] offset:6144\n\t"
        "v_mfma_f32_16x16x32_bf16 v[88:91], v[36:39], v[56:59], v[88:91]\n\t"
        "ds_read_b64_tr_b16 v[46:47], %[wa0] offset:6912\n\t"
        "v_mfma_f32_16x16x32_bf16 v[92:95], v[40:43], v[56:59], v[92:95]\n\t"
        "ds_read_b128 v[56:59], v24 offset:128\n\t"
        "v_mfma_f32_16x16x32_bf16 v[104:107], v[40:43], v[60:63], v[104:107]\n\t"
        "ds_read_b64_tr_b16 v[48:49], %[wa1] offset:6144\n\t"
        "v_mfma_f32_16x16x32_bf16 v[100:103], v[36:39], v[60:63], v[100:103]\n\t"
        "ds_read_b64_tr_b16 v[50:51], %[wa1] offset:6912\n\t"
        "v_xor_b32 v16, %[q16], v16\n\t"
        "v_mfma_f32_16x16x32_bf16 v[96:99], v[32:35], v[60:63], v[96:99]\n\t"
        "ds_read_b128 v[60:63], v25 offset:128\n\t"
        "v_xor_b32 v17, %[q16], v17\n\t"
        "v_mfma_f32_16x16x32_bf16 v[108:111], v[32:35], v[64:67], v[108:111]\n\t"
        "ds_read_b64_tr_b16 v[52:53], %[wa2] offset:6144\n\t"
        "v_xor_b32 v18, %[q16], v18\n\t"
        "v_mfma_f32_16x16x32_bf16 v[112:115], v[36:39], v[64:67], v[112:115]\n\t"
        "ds_read_b64_tr_b16 v[54:55], %[wa2] offset:6912\n\t"
        "v_xor_b32 v19, %[q16], v19\n\t"
        "v_mfma_f32_16x16x32_bf16 v[116:119], v[40:43], v[64:67], v[116:119]\n\t"
        "ds_read_b128 v[64:67], v26 offset:128\n\t"
        "v_xor_b32 v20, %[q16], v20\n\t"
        "v_mfma_f32_16x16x32_bf16 v[128:131], v[40:43], v[68:71], v[128:131]\n\t"
        "v_xor_b32 v21, %[q16], v21\n\t"
        "v_mfma_f32_16x16x32_bf16 v[124:127], v[36:39], v[68:71], v[124:127]\n\t"
        "v_xor_b32 v22, %[q16], v22\n\t"
        "v_mfma_f32_16x16x32_bf16 v[120:123], v[32:35], v[68:71], v[120:123]\n\t"
        "ds_read_b128 v[68:71], v27 offset:128\n\t"
        "s_waitcnt lgkmcnt(5)\n\t"
        "v_mfma_f32_16x16x32_bf16 v[84:87], v[44:47], v[56:59], v[84:87]\n\t"
        "ds_read_b64_tr_b16 v[32:33], %[wa0] offset:12288\n\t"
        "v_mfma_f32_16x16x32_bf16 v[88:91], v[48:51], v[56:59], v[88:91]\n\t"
        "s_barrier\n\t"
        "ds_read_b64_tr_b16 v[34:35], %[wa0] offset:13056\n\t"
        "s_waitcnt lgkmcnt(4)\n\t"
        "v_mfma_f32_16x16x32_bf16 v[92:95], v[52:55], v[56:59], v[92:95]\n\t"
        "ds_read_b128 v[56:59], v16 offset:0\n\t"
        "v_mfma_f32_16x16x32_bf16 v[104:107], v[52:55], v[60:63], v[104:107]\n\t"
        "ds_read_b64_tr_b16 v[36:37], %[wa1] offset:12288\n\t"
        "v_mfma_f32_16x16x32_bf16 v[100:103], v[48:51], v[60:63], v[100:103]\n\t"
        "ds_read_b64_tr_b16 v[38:39], %[wa1] offset:13056\n\t"
        "v_mfma_f32_16x16x32_bf16 v[96:99], v[44:47], v[60:63], v[96:99]\n\t"
        "ds_read_b128 v[60:63], v17 offset:0\n\t"
        "s_waitcnt lgkmcnt(7)\n\t"
        "v_mfma_f32_16x16x32_bf16 v[108:111], v[44:47], v[64:67], v[108:111]\n\t"
        "ds_read_b64_tr_b16 v[40:41], %[wa2] offset:12288\n\t"
        "v_mfma_f32_16x16x32_bf16 v[112:115], v[48:51], v[64:67], v[112:115]\n\t"
        "ds_read_b64_tr_b16 v[42:43], %[wa2] offset:13056\n\t"
        "v_mfma_f32_16x16x32_bf16 v[116:119], v[52:55], v[64:67], v[116:119]\n\t"
        "ds_read_b128 v[64:67], v18 offset:0\n\t"
        "s_waitcnt lgkmcnt(9)\n\t"
        "v_mfma_f32_16x16x32_bf16 v[128:131], v[52:55], v[68:71], v[128:131]\n\t"
        "v_mfma_f32_16x16x32_bf16 v[124:127], v[48:51], v[68:71], v[124:127]\n\t"
        "v_mfma_f32_16x16x32_bf16 v[120:123], v[44:47], v[68:71], v[120:123]\n\t"
        "ds_read_b128 v[68:71], v19 offset:0\n\t"
        "s_waitcnt lgkmcnt(5)\n\t"
        "v_mfma_f32_16x16x32_bf16 v[84:87], v[32:35], v[56:59], v[84:87]\n\t"
        "ds_read_b64_tr_b16 v[44:45], %[wa0] offset:18432\n\t"
        "v_mfma_f32_16x16x32_bf16 v[88:91], v[36:39], v[56:59], v[88:91]\n\t"
        "ds_read_b64_tr_b16 v[46:47], %[wa0] offset:19200\n\t"
        "s_waitcnt lgkmcnt(4)\n\t"
        "v_mfma_f32_16x16x32_bf16 v[92:95], v[40:43], v[56:59], v[92:95]\n\t"
        "ds_read_b128 v[56:59], v16 offset:64\n\t"
        "v_mfma_f32_16x16x32_bf16 v[104:107], v[40:43], v[60:63], v[104:107]\n\t"
        "ds_read_b64_tr_b16 v[48:49], %[wa1] offset:18432\n\t"
        "v_mfma_f32_16x16x32_bf16 v[100:103], v[36:39], v[60:63], v[100:103]\n\t"
        "ds_read_b64_tr_b16 v[50:51], %[wa1] offset:19200\n\t"
        "v_mfma_f32_16x16x32_bf16 v[96:99], v[32:35], v[60:63], v[96:99]\n\t"
        "ds_read_b128 v[60:63], v17 offset:64\n\t"
        "s_waitcnt lgkmcnt(7)\n\t"
        "v_mfma_f32_16x16x32_bf16 v[108:111], v[32:35], v[64:67], v[108:111]\n\t"
        "ds_read_b64_tr_b16 v[52:53], %[wa2] offset:18432\n\t"
        "v_mfma_f32_16x16x32_bf16 v[112:115], v[36:39], v[64:67], v[112:115]\n\t"
        "ds_read_b64_tr_b16 v[54:55], %[wa2] offset:19200\n\t"
        "v_mfma_f32_16x16x32_bf16 v[116:119], v[40:43], v[64:67], v[116:119]\n\t"
        "ds_read_b128 v[64:67], v18 offset:64\n\t"
        "s_waitcnt lgkmcnt(9)\n\t"
        "v_mfma_f32_16x16x32_bf16 v[128:131], v[40:43], v[68:71], v[128:131]\n\t"
        "v_mfma_f32_16x16x32_bf16 v[124:127], v[36:39], v[68:71], v[124:127]\n\t"
        "v_mfma_f32_16x16x32_bf16 v[120:123], v[32:35], v[68:71], v[120:123]\n\t"
        "ds_read_b128 v[68:71], v19 offset:64\n\t"
        "s_waitcnt lgkmcnt(5)\n\t"
        "v_mfma_f32_16x16x32_bf16 v[84:87], v[44:47], v[56:59], v[84:87]\n\t"
        "ds_read_b64_tr_b16 v[32:33], %[wa0] offset:24576\n\t"
        "v_lshlrev_b32 v12, 2, %[ta]\n\t"
        "v_mfma_f32_16x16x32_bf16 v[88:91], v[48:51], v[56:59], v[88:91]\n\t"
        "ds_read_b64_tr_b16 v[34:35], %[wa0] offset:25344\n\t"
        "v_lshrrev_b32 v13, 1, %[ta]\n\t"
        "s_waitcnt lgkmcnt(4)\n\t"
        "v_mfma_f32_16x16x32_bf16 v[92:95], v[52:55], v[56:59], v[92:95]\n\t"
        "ds_read_b128 v[56:59], v16 offset:128\n\t"
        "v_and_b32 v12, 0x3c0, v12\n\t"
        "v_mfma_f32_16x16x32_bf16 v[104:107], v[52:55], v[60:63], v[104:107]\n\t"
        "ds_read_b64_tr_b16 v[36:37], %[wa1] offset:24576\n\t"
        "v_and_or_b32 v12, v13, 48, v12\n\t"
        "v_mfma_f32_16x16x32_bf16 v[100:103], v[48:51], v[60:63], v[100:103]\n\t"
        "ds_read_b64_tr_b16 v[38:39], %[wa1] offset:25344\n\t"
        "v_and_b32 v13, 48, %[vo2]\n\t"
        "v_mfma_f32_16x16x32_bf16 v[96:99], v[44:47], v[60:63], v[96:99]\n\t"
        "ds_read_b128 v[60:63], v17 offset:128\n\t"
        "v_xor_b32 v13, v12, v13\n\t"
        "s_waitcnt lgkmcnt(7)\n\t"
        "v_mfma_f32_16x16x32_bf16 v[108:111], v[44:47], v[64:67], v[108:111]\n\t"
        "ds_read_b64_tr_b16 v[40:41], %[wa2] offset:24576\n\t"
        "s_lshl_b32 s40, %[nh], 10\n\t"
        "v_mfma_f32_16x16x32_bf16 v[112:115], v[48:51], v[64:67], v[112:115]\n\t"
        "ds_read_b64_tr_b16 v[42:43], %[wa2] offset:25344\n\t"
        "s_add_u32 s40, s40, %[xl]\n\t"
        "v_mfma_f32_16x16x32_bf16 v[116:119], v[52:55], v[64:67], v[116:119]\n\t"
        "ds_read_b128 v[64:67], v18 offset:128\n\t"
        "v_add_u32 v13, s40, v13\n\t"
        "s_waitcnt lgkmcnt(9)\n\t"
        "v_mfma_f32_16x16x32_bf16 v[128:131], v[52:55], v[68:71], v[128:131]\n\t"
        "s_lshl_b32 s40, %[nh], 5\n\t"
        "v_mfma_f32_16x16x32_bf16 v[124:127], v[48:51], v[68:71], v[124:127]\n\t"
        "v_xor_b32 v12, s40, v12\n\t"
        "v_mfma_f32_16x16x32_bf16 v[120:123], v[44:47], v[68:71], v[120:123]\n\t"
        "ds_read_b128 v[68:71], v19 offset:128\n\t"
        "v_lshrrev_b32 %[ca], 1, %[q16]\n\t"
        "s_waitcnt lgkmcnt(5)\n\t"
        "v_mfma_f32_16x16x32_bf16 v[84:87], v[32:35], v[56:59], v[84:87]\n\t"
        "ds_read_b64_tr_b16 v[44:45], %[wa0] offset:0\n\t"
        "v_xor_b32 v12, %[ca], v12\n\t"
        "v_mfma_f32_16x16x32_bf16 v[88:91], v[36:39], v[56:59], v[88:91]\n\t"
        "ds_read_b64_tr_b16 v[46:47], %[wa0] offset:768\n\t"
        "v_add_u32 v12, %[xl], v12\n\t"
        "s_waitcnt lgkmcnt(4)\n\t"
        "v_mfma_f32_16x16x32_bf16 v[92:95], v[40:43], v[56:59], v[92:95]\n\t"
        "ds_read_b64_tr_b16 v[48:49], %[wa1] offset:0\n\t"
        "v_mfma_f32_16x16x32_bf16 v[104:107], v[40:43], v[60:63], v[104:107]\n\t"
        "ds_read_b64_tr_b16 v[50:51], %[wa1] offset:768\n\t"
        "v_mfma_f32_16x16x32_bf16 v[100:103], v[36:39], v[60:63], v[100:103]\n\t"
        "ds_read_b64_tr_b16 v[52:53], %[wa2] offset:0\n\t"
        "v_mfma_f32_16x16x32_bf16 v[96:99], v[32:35], v[60:63], v[96:99]\n\t"
        "ds_read_b64_tr_b16 v[54:55], %[wa2] offset:768\n\t"
        "s_waitcnt lgkmcnt(7)\n\t"
        "v_mfma_f32_16x16x32_bf16 v[108:111], v[32:35], v[64:67], v[108:111]\n\t"
        "ds_read_b128 v[56:59], v28 offset:128\n\t"
        "v_cvt_pk_bf16_f32 v84, v84, v85\n\t"
        "v_mfma_f32_16x16x32_bf16 v[112:115], v[36:39], v[64:67], v[112:115]\n\t"
        "ds_read_b128 v[60:63], v29 offset:128\n\t"
        "v_cvt_pk_bf16_f32 v85, v86, v87\n\t"
        "v_mfma_f32_16x16x32_bf16 v[116:119], v[40:43], v[64:67], v[116:119]\n\t"
        "ds_read_b128 v[64:67], v30 offset:128\n\t"
        "v_cvt_pk_bf16_f32 v86, v88, v89\n\t"
        "s_waitcnt lgkmcnt(9)\n\t"
        "v_mfma_f32_16x16x32_bf16 v[128:131], v[40:43], v[68:71], v[128:131]\n\t"
        "v_cvt_pk_bf16_f32 v87, v90, v91\n\t"
        "s_mov_b32 s40, 0\n\t"
        "v_mfma_f32_16x16x32_bf16 v[124:127], v[36:39], v[68:71], v[124:127]\n\t"
        "v_permlane16_swap_b32 v84, v86\n\t"
        "v_permlane16_swap_b32 v85, v87\n\t"
        "v_mfma_f32_16x16x32_bf16 v[120:123], v[32:35], v[68:71], v[120:123]\n\t"
        "ds_read_b64_tr_b16 v[32:33], %[wa0] offset:6144\n\t"
        "buffer_store_dwordx4 v[84:87], %[vo], %[ro], s40 offen sc1\n\t"
        "s_waitcnt lgkmcnt(6)\n\t"
        "v_mfma_f32_16x16x32_bf16 v[132:135], v[44:47], v[72:75], v[132:135]\n\t"
        "ds_read_b64_tr_b16 v[34:35], %[wa0] offset:6912\n\t"
        "v_cvt_pk_bf16_f32 v92, v92, v93\n\t"
        "v_mfma_f32_16x16x32_bf16 v[136:139], v[48:51], v[72:75], v[136:139]\n\t"
        "ds_read_b64_tr_b16 v[36:37], %[wa1] offset:6144\n\t"
        "v_cvt_pk_bf16_f32 v93, v94, v95\n\t"
        "s_waitcnt lgkmcnt(6)\n\t"
        "v_mfma_f32_16x16x32_bf16 v[140:143], v[52:55], v[72:75], v[140:143]\n\t"
        "ds_read_b64_tr_b16 v[38:39], %[wa1] offset:6912\n\t"
        "ds_write_b64 v12, v[92:93] offset:0\n\t"
        "v_mfma_f32_16x16x32_bf16 v[152:155], v[52:55], v[76:79], v[152:155]\n\t"
        "ds_read_b64_tr_b16 v[40:41], %[wa2] offset:6144\n\t"
        "v_cvt_pk_bf16_f32 v104, v104, v105\n\t"
        "v_mfma_f32_16x16x32_bf16 v[148:151], v[48:51], v[76:79], v[148:151]\n\t"
        "ds_read_b64_tr_b16 v[42:43], %[wa2] offset:6912\n\t"
        "v_cvt_pk_bf16_f32 v105, v106, v107\n\t"
        "v_mfma_f32_16x16x32_bf16 v[144:147], v[44:47], v[76:79], v[144:147]\n\t"
        "ds_read_b128 v[72:75], v20 offset:0\n\t"
        "ds_write_b64 v12, v[104:105] offset:1024\n\t"
        "v_mfma_f32_16x16x32_bf16 v[156:159], v[44:47], v[80:83], v[156:159]\n\t"
        "ds_read_b128 v[76:79], v21 offset:0\n\t"
        "v_cvt_pk_bf16_f32 v96, v96, v97\n\t"
        "v_mfma_f32_16x16x32_bf16 v[160:163], v[48:51], v[80:83], v[160:163]\n\t"
        "v_cvt_pk_bf16_f32 v97, v98, v99\n\t"
        "v_cvt_pk_bf16_f32 v98, v100, v101\n\t"
        "v_mfma_f32_16x16x32_bf16 v[164:167], v[52:55], v[80:83], v[164:167]\n\t"
        "ds_read_b64_tr_b16 v[44:45], %[wa0] offset:12288\n\t"
        "v_cvt_pk_bf16_f32 v99, v102, v103\n\t"
        "s_waitcnt lgkmcnt(7)\n\t"
        "v_mfma_f32_16x16x32_bf16 v[132:135], v[32:35], v[56:59], v[132:135]\n\t"
        "ds_read_b64_tr_b16 v[46:47], %[wa0] offset:13056\n\t"
        "s_mov_b32 s40, 43008\n\t"
        "v_mfma_f32_16x16x32_bf16 v[136:139], v[36:39], v[56:59], v[136:139]\n\t"
        "ds_read_b64_tr_b16 v[48:49], %[wa1] offset:12288\n\t"
        "v_permlane16_swap_b32 v96, v98\n\t"
        "s_waitcnt lgkmcnt(6)\n\t"
        "v_mfma_f32_16x16x32_bf16 v[140:143], v[40:43], v[56:59], v[140:143]\n\t"
        "ds_read_b64_tr_b16 v[50:51], %[wa1] offset:13056\n\t"
        "v_permlane16_swap_b32 v97, v99\n\t"
        "v_mfma_f32_16x16x32_bf16 v[152:155], v[40:43], v[60:63], v[152:155]\n\t"
        "ds_read_b64_tr_b16 v[52:53], %[wa2] offset:12288\n\t"
        "buffer_store_dwordx4 v[96:99], %[vo], %[ro], s40 offen sc1\n\t"
        "v_mfma_f32_16x16x32_bf16 v[148:151], v[36:39], v[60:63], v[148:151]\n\t"
        "ds_read_b64_tr_b16 v[54:55], %[wa2] offset:13056\n\t"
        "v_cvt_pk_bf16_f32 v116, v116, v117\n\t"
        "v_mfma_f32_16x16x32_bf16 v[144:147], v[32:35], v[60:63], v[144:147]\n\t"
        "ds_read_b128 v[80:83], v22 offset:0\n\t"
        "v_cvt_pk_bf16_f32 v117, v118, v119\n\t"
        "v_mfma_f32_16x16x32_bf16 v[156:159], v[32:35], v[64:67], v[156:159]\n\t"
        "ds_read_b128 v[56:59], v20 offset:64\n\t"
        "ds_write_b64 v12, v[116:117] offset:2048\n\t"
        "v_mfma_f32_16x16x32_bf16 v[160:163], v[36:39], v[64:67], v[160:163]\n\t"
        "ds_read_b128 v[60:63], v21 offset:64\n\t"
        "v_cvt_pk_bf16_f32 v128, v128, v129\n\t"
        "v_mfma_f32_16x16x32_bf16 v[164:167], v[40:43], v[64:67], v[164:167]\n\t"
        "ds_read_b64_tr_b16 v[32:33], %[wa0] offset:18432\n\t"
        "v_cvt_pk_bf16_f32 v129, v130, v131\n\t"
        "s_waitcnt lgkmcnt(7)\n\t"
        "v_mfma_f32_16x16x32_bf16 v[132:135], v[44:47], v[72:75], v[132:135]\n\t"
        "ds_read_b64_tr_b16 v[34:35], %[wa0] offset:19200\n\t"
        "ds_write_b64 v12, v[128:129] offset:3072\n\t"
        "v_mfma_f32_16x16x32_bf16 v[136:139], v[48:51], v[72:75], v[136:139]\n\t"
        "ds_read_b64_tr_b16 v[36:37], %[wa1] offset:18432\n\t"
        "v_cvt_pk_bf16_f32 v108, v108, v109\n\t"
        "s_waitcnt lgkmcnt(8)\n\t"
        "v_mfma_f32_16x16x32_bf16 v[140:143], v[52:55], v[72:75], v[140:143]\n\t"
        "ds_read_b64_tr_b16 v[38:39], %[wa1] offset:19200\n\t"
        "v_cvt_pk_bf16_f32 v109, v110, v111\n\t"
        "v_mfma_f32_16x16x32_bf16 v[152:155], v[52:55], v[76:79], v[152:155]\n\t"
        "ds_read_b64_tr_b16 v[40:41], %[wa2] offset:18432\n\t"
        "v_cvt_pk_bf16_f32 v110, v112, v113\n\t"
        "v_mfma_f32_16x16x32_bf16 v[148:151], v[48:51], v[76:79], v[148:151]\n\t"
        "ds_read_b64_tr_b16 v[42:43], %[wa2] offset:19200\n\t"
        "v_cvt_pk_bf16_f32 v111, v114, v115\n\t"
        "v_mfma_f32_16x16x32_bf16 v[144:147], v[44:47], v[76:79], v[144:147]\n\t"
        "ds_read_b128 v[64:67], v22 offset:64\n\t"
        "s_mov_b32 s40, 86016\n\t"
        "s_waitcnt lgkmcnt(11)\n\t"
        "v_mfma_f32_16x16x32_bf16 v[156:159], v[44:47], v[80:83], v[156:159]\n\t"
        "ds_read_b128 v[72:75], v20 offset:128\n\t"
        "v_permlane16_swap_b32 v108, v110\n\t"
        "v_mfma_f32_16x16x32_bf16 v[160:163], v[48:51], v[80:83], v[160:163]\n\t"
        "ds_read_b128 v[76:79], v21 offset:128\n\t"
        "v_permlane16_swap_b32 v109, v111\n\t"
        "v_mfma_f32_16x16x32_bf16 v[164:167], v[52:55], v[80:83], v[164:167]\n\t"
        "ds_read_b64_tr_b16 v[44:45], %[wa0] offset:24576\n\t"
        "buffer_store_dwordx4 v[108:111], %[vo], %[ro], s40 offen sc1\n\t"
        "s_waitcnt lgkmcnt(6)\n\t"
        "v_mfma_f32_16x16x32_bf16 v[132:135], v[32:35], v[56:59], v[132:135]\n\t"
        "ds_read_b64_tr_b16 v[46:47], %[wa0] offset:25344\n\t"
        "v_mfma_f32_16x16x32_bf16 v[136:139], v[36:39], v[56:59], v[136:139]\n\t"
        "ds_read_b64_tr_b16 v[48:49], %[wa1] offset:24576\n\t"
        "s_barrier\n\t"
        "s_waitcnt lgkmcnt(6)\n\t"
        "v_mfma_f32_16x16x32_bf16 v[140:143], v[40:43], v[56:59], v[140:143]\n\t"
        "ds_read_b64_tr_b16 v[50:51], %[wa1] offset:25344\n\t"
        "ds_read_b128 v[92:95], v13 offset:0\n\t"
        "v_mfma_f32_16x16x32_bf16 v[152:155], v[40:43], v[60:63], v[152:155]\n\t"
        "ds_read_b64_tr_b16 v[52:53], %[wa2] offset:24576\n\t"
        "ds_read_b128 v[116:119], v13 offset:2048\n\t"
        "v_mfma_f32_16x16x32_bf16 v[148:151], v[36:39], v[60:63], v[148:151]\n\t"
        "ds_read_b64_tr_b16 v[54:55], %[wa2] offset:25344\n\t"
        "v_cvt_pk_bf16_f32 v120, v120, v121\n\t"
        "v_mfma_f32_16x16x32_bf16 v[144:147], v[32:35], v[60:63], v[144:147]\n\t"
        "ds_read_b128 v[80:83], v22 offset:128\n\t"
        "v_cvt_pk_bf16_f32 v121, v122, v123\n\t"
        "s_waitcnt lgkmcnt(11)\n\t"
        "v_mfma_f32_16x16x32_bf16 v[156:159], v[32:35], v[64:67], v[156:159]\n\t"
        "v_cvt_pk_bf16_f32 v122, v124, v125\n\t"
        "v_cvt_pk_bf16_f32 v123, v126, v127\n\t"
        "v_mfma_f32_16x16x32_bf16 v[160:163], v[36:39], v[64:67], v[160:163]\n\t"
        "s_mov_b32 s40, 129024\n\t"
        "v_permlane16_swap_b32 v120, v122\n\t"
        "v_mfma_f32_16x16x32_bf16 v[164:167], v[40:43], v[64:67], v[164:167]\n\t"
        "v_permlane16_swap_b32 v121, v123\n\t"
        "buffer_store_dwordx4 v[120:123], %[vo], %[ro], s40 offen sc1\n\t"
        "s_waitcnt lgkmcnt(5)\n\t"
        "v_mfma_f32_16x16x32_bf16 v[132:135], v[44:47], v[72:75], v[132:135]\n\t"
        "s_mov_b32 s40, 0\n\t"
        "s_waitcnt lgkmcnt(4)\n\t"
        "v_mfma_f32_16x16x32_bf16 v[136:139], v[48:51], v[72:75], v[136:139]\n\t"
        "buffer_store_dwordx4 v[92:95], %[vo2], %[ro], s40 offen sc1\n\t"
        "s_mov_b32 s40, 86016\n\t"
        "s_waitcnt lgkmcnt(1)\n\t"
        "v_mfma_f32_16x16x32_bf16 v[140:143], v[52:55], v[72:75], v[140:143]\n\t"
        "buffer_store_dwordx4 v[116:119], %[vo2], %[ro], s40 offen sc1\n\t"
        "v_mfma_f32_16x16x32_bf16 v[152:155], v[52:55], v[76:79], v[152:155]\n\t"
        "s_barrier\n\t"
        "v_mfma_f32_16x16x32_bf16 v[148:151], v[48:51], v[76:79], v[148:151]\n\t"
        "v_mfma_f32_16x16x32_bf16 v[144:147], v[44:47], v[76:79], v[144:147]\n\t"
        "s_waitcnt lgkmcnt(0)\n\t"
        "v_mfma_f32_16x16x32_bf16 v[156:159], v[44:47], v[80:83], v[156:159]\n\t"
        "v_mfma_f32_16x16x32_bf16 v[160:163], v[48:51], v[80:83], v[160:163]\n\t"
        "v_mfma_f32_16x16x32_bf16 v[164:167], v[52:55], v[80:83], v[164:167]\n\t"
        "s_nop 7\n\t"
        "v_cvt_pk_bf16_f32 v140, v140, v141\n\t"
        "v_cvt_pk_bf16_f32 v141, v142, v143\n\t"
        "ds_write_b64 v12, v[140:141] offset:0\n\t"
        "v_cvt_pk_bf16_f32 v152, v152, v153\n\t"
        "v_cvt_pk_bf16_f32 v153, v154, v155\n\t"
        "ds_write_b64 v12, v[152:153] offset:1024\n\t"
        "v_cvt_pk_bf16_f32 v164, v164, v165\n\t"
        "v_cvt_pk_bf16_f32 v165, v166, v167\n\t"
        "ds_write_b64 v12, v[164:165] offset:2048\n\t"
        "v_cvt_pk_bf16_f32 v132, v132, v133\n\t"
        "v_cvt_pk_bf16_f32 v133, v134, v135\n\t"
        "v_cvt_pk_bf16_f32 v134, v136, v137\n\t"
        "v_cvt_pk_bf16_f32 v135, v138, v139\n\t"
        "s_mov_b32 s40, 172032\n\t"
        "s_nop 0\n\t"
        "v_permlane16_swap_b32 v132, v134\n\t"
        "v_permlane16_swap_b32 v133, v135\n\t"
        "buffer_store_dwordx4 v[132:135], %[vo], %[ro], s40 offen sc1\n\t"
        "s_waitcnt lgkmcnt(0)\n\t"
        "s_barrier\n\t"
        "ds_read_b128 v[140:143], v13 offset:0\n\t"
        "ds_read_b128 v[164:167], v13 offset:2048\n\t"
        "v_cvt_pk_bf16_f32 v144, v144, v145\n\t"
        "v_cvt_pk_bf16_f32 v145, v146, v147\n\t"
        "v_cvt_pk_bf16_f32 v146, v148, v149\n\t"
        "v_cvt_pk_bf16_f32 v147, v150, v151\n\t"
        "s_mov_b32 s40, 215040\n\t"
        "s_nop 0\n\t"
        "v_permlane16_swap_b32 v144, v146\n\t"
        "v_permlane16_swap_b32 v145, v147\n\t"
        "buffer_store_dwordx4 v[144:147], %[vo], %[ro], s40 offen sc1\n\t"
        "v_cvt_pk_bf16_f32 v156, v156, v157\n\t"
        "v_cvt_pk_bf16_f32 v157, v158, v159\n\t"
        "v_cvt_pk_bf16_f32 v158, v160, v161\n\t"
        "v_cvt_pk_bf16_f32 v159, v162, v163\n\t"
        "s_mov_b32 s40, 258048\n\t"
        "s_nop 0\n\t"
        "v_permlane16_swap_b32 v156, v158\n\t"
        "v_permlane16_swap_b32 v157, v159\n\t"
        "buffer_store_dwordx4 v[156:159], %[vo], %[ro], s40 offen sc1\n\t"
        "s_mov_b32 s40, 172032\n\t"
        "s_waitcnt lgkmcnt(1)\n\t"
        "buffer_store_dwordx4 v[140:143], %[vo2], %[ro], s40 offen sc1\n\t"
        "s_cmp_lg_u32 %[nh], 0\n\t"
        "s_cbranch_scc1 8f\n\t"
        "s_mov_b32 s40, 258048\n\t"
        "s_waitcnt lgkmcnt(0)\n\t"
        "buffer_store_dwordx4 v[164:167], %[vo2], %[ro], s40 offen sc1\n\t"
        "s_branch 8f\n\t"
        "65:\n\t"
        "s_setprio 0\n\t"
        "s_waitcnt lgkmcnt(0)\n\t"
        ".p2align 6\n\t"
        "60:\n\t"
        "v_mfma_f32_16x16x32_bf16 v[84:87], v[32:35], v[56:59], v[84:87]\n\t"
        "ds_read_b64_tr_b16 v[44:45], %[wa0] offset:18432\n\t"
        "v_mfma_f32_16x16x32_bf16 v[88:91], v[36:39], v[56:59], v[88:91]\n\t"
        "ds_read_b64_tr_b16 v[46:47], %[wa0] offset:19200\n\t"
        "v_mfma_f32_16x16x32_bf16 v[92:95], v[40:43], v[56:59], v[92:95]\n\t"
        "ds_read_b128 v[56:59], v24 offset:64\n\t"
        "s_waitcnt lgkmcnt(7)\n\t"
        "v_mfma_f32_16x16x32_bf16 v[104:107], v[40:43], v[60:63], v[104:107]\n\t"
        "ds_read_b64_tr_b16 v[48:49], %[wa1] offset:18432\n\t"
        "v_mfma_f32_16x16x32_bf16 v[100:103], v[36:39], v[60:63], v[100:103]\n\t"
        "ds_read_b64_tr_b16 v[50:51], %[wa1] offset:19200\n\t"
        "v_mfma_f32_16x16x32_bf16 v[96:99], v[32:35], v[60:63], v[96:99]\n\t"
        "ds_read_b128 v[60:63], v25 offset:64\n\t"
        "s_waitcnt lgkmcnt(9)\n\t"
        "v_mfma_f32_16x16x32_bf16 v[108:111], v[32:35], v[64:67], v[108:111]\n\t"
        "ds_read_b64_tr_b16 v[52:53], %[wa2] offset:18432\n\t"
        "v_mfma_f32_16x16x32_bf16 v[112:115], v[36:39], v[64:67], v[112:115]\n\t"
        "ds_read_b64_tr_b16 v[54:55], %[wa2] offset:19200\n\t"
        "v_mfma_f32_16x16x32_bf16 v[116:119], v[40:43], v[64:67], v[116:119]\n\t"
        "ds_read_b128 v[64:67], v26 offset:64\n\t"
        "s_waitcnt lgkmcnt(11)\n\t"
        "v_mfma_f32_16x16x32_bf16 v[128:131], v[40:43], v[68:71], v[128:131]\n\t"
        "ds_read2_b32 v[14:15], %[ca] offset0:0 offset1:1\n\t"
        "v_mfma_f32_16x16x32_bf16 v[124:127], v[36:39], v[68:71], v[124:127]\n\t"
        "ds_read_b128 v[16:19], %[ta] offset:1024\n\t"
        "v_mfma_f32_16x16x32_bf16 v[120:123], v[32:35], v[68:71], v[120:123]\n\t"
        "ds_read_b128 v[68:71], v27 offset:64\n\t"
        "s_waitcnt lgkmcnt(13)\n\t"
        "v_mfma_f32_16x16x32_bf16 v[132:135], v[32:35], v[72:75], v[132:135]\n\t"
        "ds_read_b128 v[20:23], %[ta] offset:1280\n\t"
        "v_mfma_f32_16x16x32_bf16 v[136:139], v[36:39], v[72:75], v[136:139]\n\t"
        "v_mfma_f32_16x16x32_bf16 v[140:143], v[40:43], v[72:75], v[140:143]\n\t"
        "ds_read_b128 v[72:75], v28 offset:64\n\t"
        "s_waitcnt lgkmcnt(14)\n\t"
        "v_mfma_f32_16x16x32_bf16 v[152:155], v[40:43], v[76:79], v[152:155]\n\t"
        "v_mfma_f32_16x16x32_bf16 v[148:151], v[36:39], v[76:79], v[148:151]\n\t"
        "s_waitcnt lgkmcnt(6)\n\t"
        "v_mfma_f32_16x16x32_bf16 v[144:147], v[32:35], v[76:79], v[144:147]\n\t"
        "ds_read_b128 v[76:79], v29 offset:64\n\t"
        "v_mfma_f32_16x16x32_bf16 v[84:87], v[44:47], v[56:59], v[84:87]\n\t"
        "ds_read_b64_tr_b16 v[32:33], %[wa0] offset:24576\n\t"
        "v_mfma_f32_16x16x32_bf16 v[88:91], v[48:51], v[56:59], v[88:91]\n\t"
        "ds_read_b64_tr_b16 v[34:35], %[wa0] offset:25344\n\t"
        "s_waitcnt lgkmcnt(4)\n\t"
        "v_mfma_f32_16x16x32_bf16 v[92:95], v[52:55], v[56:59], v[92:95]\n\t"
        "ds_read_b128 v[56:59], v24 offset:128\n\t"
        "v_add_u32 v14, v14, v15\n\t"
        "v_mfma_f32_16x16x32_bf16 v[104:107], v[52:55], v[60:63], v[104:107]\n\t"
        "ds_read_b64_tr_b16 v[36:37], %[wa1] offset:24576\n\t"
        "v_cmp_lt_u16 vcc, 208, v14\n\t"
        "v_mfma_f32_16x16x32_bf16 v[100:103], v[48:51], v[60:63], v[100:103]\n\t"
        "ds_read_b64_tr_b16 v[38:39], %[wa1] offset:25344\n\t"
        "v_xor_b32 v24, %[q16], v16\n\t"
        "v_mfma_f32_16x16x32_bf16 v[96:99], v[44:47], v[60:63], v[96:99]\n\t"
        "ds_read_b128 v[60:63], v25 offset:128\n\t"
        "v_mfma_f32_16x16x32_bf16 v[108:111], v[44:47], v[64:67], v[108:111]\n\t"
        "ds_read_b64_tr_b16 v[40:41], %[wa2] offset:24576\n\t"
        "v_xor_b32 v25, %[q16], v17\n\t"
        "v_mfma_f32_16x16x32_bf16 v[112:115], v[48:51], v[64:67], v[112:115]\n\t"
        "ds_read_b64_tr_b16 v[42:43], %[wa2] offset:25344\n\t"
        "v_mfma_f32_16x16x32_bf16 v[116:119], v[52:55], v[64:67], v[116:119]\n\t"
        "ds_read_b128 v[64:67], v26 offset:128\n\t"
        "v_mfma_f32_16x16x32_bf16 v[128:131], v[52:55], v[68:71], v[128:131]\n\t"
        "v_xor_b32 v26, %[q16], v18\n\t"
        "v_mfma_f32_16x16x32_bf16 v[124:127], v[48:51], v[68:71], v[124:127]\n\t"
        "s_waitcnt lgkmcnt(10)\n\t"
        "v_mfma_f32_16x16x32_bf16 v[120:123], v[44:47], v[68:71], v[120:123]\n\t"
        "ds_read_b128 v[68:71], v27 offset:128\n\t"
        "v_mfma_f32_16x16x32_bf16 v[132:135], v[44:47], v[72:75], v[132:135]\n\t"
        "v_xor_b32 v27, %[q16], v19\n\t"
        "v_mfma_f32_16x16x32_bf16 v[136:139], v[48:51], v[72:75], v[136:139]\n\t"
        "s_waitcnt lgkmcnt(10)\n\t"
        "v_mfma_f32_16x16x32_bf16 v[140:143], v[52:55], v[72:75], v[140:143]\n\t"
        "ds_read_b128 v[72:75], v28 offset:128\n\t"
        "v_mfma_f32_16x16x32_bf16 v[152:155], v[52:55], v[76:79], v[152:155]\n\t"
        "v_xor_b32 v28, %[q16], v20\n\t"
        "v_mfma_f32_16x16x32_bf16 v[148:151], v[48:51], v[76:79], v[148:151]\n\t"
        "s_waitcnt lgkmcnt(3)\n\t"
        "v_mfma_f32_16x16x32_bf16 v[144:147], v[44:47], v[76:79], v[144:147]\n\t"
        "ds_read_b128 v[76:79], v29 offset:128\n\t"
        "v_mfma_f32_16x16x32_bf16 v[84:87], v[32:35], v[56:59], v[84:87]\n\t"
        "ds_read_b64_tr_b16 v[44:45], %[wa0] offset:30720\n\t"
        "v_xor_b32 v29, %[q16], v21\n\t"
        "v_mfma_f32_16x16x32_bf16 v[88:91], v[36:39], v[56:59], v[88:91]\n\t"
        "ds_read_b64_tr_b16 v[46:47], %[wa0] offset:31488\n\t"
        "v_mfma_f32_16x16x32_bf16 v[92:95], v[40:43], v[56:59], v[92:95]\n\t"
        "ds_read_b64_tr_b16 v[48:49], %[wa1] offset:30720\n\t"
        "v_mfma_f32_16x16x32_bf16 v[104:107], v[40:43], v[60:63], v[104:107]\n\t"
        "ds_read_b64_tr_b16 v[50:51], %[wa1] offset:31488\n\t"
        "v_mfma_f32_16x16x32_bf16 v[100:103], v[36:39], v[60:63], v[100:103]\n\t"
        "ds_read_b64_tr_b16 v[52:53], %[wa2] offset:30720\n\t"
        "s_waitcnt lgkmcnt(8)\n\t"
        "v_mfma_f32_16x16x32_bf16 v[96:99], v[32:35], v[60:63], v[96:99]\n\t"
        "ds_read_b64_tr_b16 v[54:55], %[wa2] offset:31488\n\t"
        "v_mfma_f32_16x16x32_bf16 v[108:111], v[32:35], v[64:67], v[108:111]\n\t"
        "v_mfma_f32_16x16x32_bf16 v[112:115], v[36:39], v[64:67], v[112:115]\n\t"
        "s_waitcnt lgkmcnt(8)\n\t"
        "v_mfma_f32_16x16x32_bf16 v[116:119], v[40:43], v[64:67], v[116:119]\n\t"
        "s_waitcnt lgkmcnt(0)\n\t"
        "s_barrier\n\t"
        "s_cbranch_vccnz 601f\n\t"
        "602:\n\t"
        "v_mfma_f32_16x16x32_bf16 v[128:131], v[40:43], v[68:71], v[128:131]\n\t"
        "ds_read_b128 v[56:59], v24 offset:0\n\t"
        "v_mfma_f32_16x16x32_bf16 v[124:127], v[36:39], v[68:71], v[124:127]\n\t"
        "ds_read_b128 v[60:63], v25 offset:0\n\t"
        "v_mfma_f32_16x16x32_bf16 v[120:123], v[32:35], v[68:71], v[120:123]\n\t"
        "ds_read_b128 v[64:67], v26 offset:0\n\t"
        "v_mfma_f32_16x16x32_bf16 v[132:135], v[32:35], v[72:75], v[132:135]\n\t"
        "ds_read_b128 v[68:71], v27 offset:0\n\t"
        "v_mfma_f32_16x16x32_bf16 v[136:139], v[36:39], v[72:75], v[136:139]\n\t"
        "v_mfma_f32_16x16x32_bf16 v[140:143], v[40:43], v[72:75], v[140:143]\n\t"
        "ds_read_b128 v[72:75], v28 offset:0\n\t"
        "v_mfma_f32_16x16x32_bf16 v[152:155], v[40:43], v[76:79], v[152:155]\n\t"
        "v_mfma_f32_16x16x32_bf16 v[148:151], v[36:39], v[76:79], v[148:151]\n\t"
        "s_sub_u32 %[tn], %[tn], 1\n\t"
        "v_mfma_f32_16x16x32_bf16 v[144:147], v[32:35], v[76:79], v[144:147]\n\t"
        "ds_read_b128 v[76:79], v29 offset:0\n\t"
        "s_cbranch_scc1 9f\n\t"
        "s_waitcnt lgkmcnt(5)\n\t"
        "v_mfma_f32_16x16x32_bf16 v[84:87], v[44:47], v[56:59], v[84:87]\n\t"
        "ds_read_b64_tr_b16 v[32:33], %[wa0] offset:0\n\t"
        "v_mfma_f32_16x16x32_bf16 v[88:91], v[48:51], v[56:59], v[88:91]\n\t"
        "ds_read_b64_tr_b16 v[34:35], %[wa0] offset:768\n\t"
        "v_mfma_f32_16x16x32_bf16 v[92:95], v[52:55], v[56:59], v[92:95]\n\t"
        "ds_read_b128 v[56:59], v24 offset:64\n\t"
        "s_waitcnt lgkmcnt(7)\n\t"
        "v_mfma_f32_16x16x32_bf16 v[104:107], v[52:55], v[60:63], v[104:107]\n\t"
        "ds_read_b64_tr_b16 v[36:37], %[wa1] offset:0\n\t"
        "v_mfma_f32_16x16x32_bf16 v[100:103], v[48:51], v[60:63], v[100:103]\n\t"
        "ds_read_b64_tr_b16 v[38:39], %[wa1] offset:768\n\t"
        "v_mfma_f32_16x16x32_bf16 v[96:99], v[44:47], v[60:63], v[96:99]\n\t"
        "ds_read_b128 v[60:63], v25 offset:64\n\t"
        "s_waitcnt lgkmcnt(9)\n\t"
        "v_mfma_f32_16x16x32_bf16 v[108:111], v[44:47], v[64:67], v[108:111]\n\t"
        "ds_read_b64_tr_b16 v[40:41], %[wa2] offset:0\n\t"
        "v_mfma_f32_16x16x32_bf16 v[112:115], v[48:51], v[64:67], v[112:115]\n\t"
        "ds_read_b64_tr_b16 v[42:43], %[wa2] offset:768\n\t"
        "v_mfma_f32_16x16x32_bf16 v[116:119], v[52:55], v[64:67], v[116:119]\n\t"
        "ds_read_b128 v[64:67], v26 offset:64\n\t"
        "s_waitcnt lgkmcnt(11)\n\t"
        "v_mfma_f32_16x16x32_bf16 v[128:131], v[52:55], v[68:71], v[128:131]\n\t"
        "ds_read2_b32 v[14:15], %[ca] offset0:1 offset1:2\n\t"
        "v_mfma_f32_16x16x32_bf16 v[124:127], v[48:51], v[68:71], v[124:127]\n\t"
        "ds_read_b128 v[16:19], %[ta] offset:0\n\t"
        "v_mfma_f32_16x16x32_bf16 v[120:123], v[44:47], v[68:71], v[120:123]\n\t"
        "ds_read_b128 v[68:71], v27 offset:64\n\t"
        "s_waitcnt lgkmcnt(13)\n\t"
        "v_mfma_f32_16x16x32_bf16 v[132:135], v[44:47], v[72:75], v[132:135]\n\t"
        "ds_read_b128 v[20:23], %[ta] offset:256\n\t"
        "v_mfma_f32_16x16x32_bf16 v[136:139], v[48:51], v[72:75], v[136:139]\n\t"
        "v_mfma_f32_16x16x32_bf16 v[140:143], v[52:55], v[72:75], v[140:143]\n\t"
        "ds_read_b128 v[72:75], v28 offset:64\n\t"
        "s_waitcnt lgkmcnt(14)\n\t"
        "v_mfma_f32_16x16x32_bf16 v[152:155], v[52:55], v[76:79], v[152:155]\n\t"
        "v_mfma_f32_16x16x32_bf16 v[148:151], v[48:51], v[76:79], v[148:151]\n\t"
        "s_waitcnt lgkmcnt(6)\n\t"
        "v_mfma_f32_16x16x32_bf16 v[144:147], v[44:47], v[76:79], v[144:147]\n\t"
        "ds_read_b128 v[76:79], v29 offset:64\n\t"
        "v_mfma_f32_16x16x32_bf16 v[84:87], v[32:35], v[56:59], v[84:87]\n\t"
        "ds_read_b64_tr_b16 v[44:45], %[wa0] offset:6144\n\t"
        "v_mfma_f32_16x16x32_bf16 v[88:91], v[36:39], v[56:59], v[88:91]\n\t"
        "ds_read_b64_tr_b16 v[46:47], %[wa0] offset:6912\n\t"
        "s_waitcnt lgkmcnt(4)\n\t"
        "v_mfma_f32_16x16x32_bf16 v[92:95], v[40:43], v[56:59], v[92:95]\n\t"
        "ds_read_b128 v[56:59], v24 offset:128\n\t"
        "v_add_u32 v14, v14, v15\n\t"
        "v_mfma_f32_16x16x32_bf16 v[104:107], v[40:43], v[60:63], v[104:107]\n\t"
        "ds_read_b64_tr_b16 v[48:49], %[wa1] offset:6144\n\t"
        "v_cmp_lt_u16 vcc, 208, v14\n\t"
        "v_mfma_f32_16x16x32_bf16 v[100:103], v[36:39], v[60:63], v[100:103]\n\t"
        "ds_read_b64_tr_b16 v[50:51], %[wa1] offset:6912\n\t"
        "v_add_u32 %[ca], 8, %[ca]\n\t"
        "v_mfma_f32_16x16x32_bf16 v[96:99], v[32:35], v[60:63], v[96:99]\n\t"
        "ds_read_b128 v[60:63], v25 offset:128\n\t"
        "v_xor_b32 v24, %[q16], v16\n\t"
        "v_mfma_f32_16x16x32_bf16 v[108:111], v[32:35], v[64:67], v[108:111]\n\t"
        "ds_read_b64_tr_b16 v[52:53], %[wa2] offset:6144\n\t"
        "v_xor_b32 v25, %[q16], v17\n\t"
        "v_mfma_f32_16x16x32_bf16 v[112:115], v[36:39], v[64:67], v[112:115]\n\t"
        "ds_read_b64_tr_b16 v[54:55], %[wa2] offset:6912\n\t"
        "v_mfma_f32_16x16x32_bf16 v[116:119], v[40:43], v[64:67], v[116:119]\n\t"
        "ds_read_b128 v[64:67], v26 offset:128\n\t"
        "v_mfma_f32_16x16x32_bf16 v[128:131], v[40:43], v[68:71], v[128:131]\n\t"
        "v_xor_b32 v26, %[q16], v18\n\t"
        "v_mfma_f32_16x16x32_bf16 v[124:127], v[36:39], v[68:71], v[124:127]\n\t"
        "s_waitcnt lgkmcnt(10)\n\t"
        "v_mfma_f32_16x16x32_bf16 v[120:123], v[32:35], v[68:71], v[120:123]\n\t"
        "ds_read_b128 v[68:71], v27 offset:128\n\t"
        "v_mfma_f32_16x16x32_bf16 v[132:135], v[32:35], v[72:75], v[132:135]\n\t"
        "v_xor_b32 v27, %[q16], v19\n\t"
        "v_mfma_f32_16x16x32_bf16 v[136:139], v[36:39], v[72:75], v[136:139]\n\t"
        "s_waitcnt lgkmcnt(10)\n\t"
        "v_mfma_f32_16x16x32_bf16 v[140:143], v[40:43], v[72:75], v[140:143]\n\t"
        "ds_read_b128 v[72:75], v28 offset:128\n\t"
        "v_mfma_f32_16x16x32_bf16 v[152:155], v[40:43], v[76:79], v[152:155]\n\t"
        "v_xor_b32 v28, %[q16], v20\n\t"
        "v_mfma_f32_16x16x32_bf16 v[148:151], v[36:39], v[76:79], v[148:151]\n\t"
        "s_waitcnt lgkmcnt(3)\n\t"
        "v_mfma_f32_16x16x32_bf16 v[144:147], v[32:35], v[76:79], v[144:147]\n\t"
        "ds_read_b128 v[76:79], v29 offset:128\n\t"
        "v_mfma_f32_16x16x32_bf16 v[84:87], v[44:47], v[56:59], v[84:87]\n\t"
        "ds_read_b64_tr_b16 v[32:33], %[wa0] offset:12288\n\t"
        "v_xor_b32 v29, %[q16], v21\n\t"
        "v_mfma_f32_16x16x32_bf16 v[88:91], v[48:51], v[56:59], v[88:91]\n\t"
        "ds_read_b64_tr_b16 v[34:35], %[wa0] offset:13056\n\t"
        "v_mfma_f32_16x16x32_bf16 v[92:95], v[52:55], v[56:59], v[92:95]\n\t"
        "ds_read_b64_tr_b16 v[36:37], %[wa1] offset:12288\n\t"
        "v_mfma_f32_16x16x32_bf16 v[104:107], v[52:55], v[60:63], v[104:107]\n\t"
        "ds_read_b64_tr_b16 v[38:39], %[wa1] offset:13056\n\t"
        "v_mfma_f32_16x16x32_bf16 v[100:103], v[48:51], v[60:63], v[100:103]\n\t"
        "ds_read_b64_tr_b16 v[40:41], %[wa2] offset:12288\n\t"
        "s_waitcnt lgkmcnt(8)\n\t"
        "v_mfma_f32_16x16x32_bf16 v[96:99], v[44:47], v[60:63], v[96:99]\n\t"
        "ds_read_b64_tr_b16 v[42:43], %[wa2] offset:13056\n\t"
        "v_mfma_f32_16x16x32_bf16 v[108:111], v[44:47], v[64:67], v[108:111]\n\t"
        "v_mfma_f32_16x16x32_bf16 v[112:115], v[48:51], v[64:67], v[112:115]\n\t"
        "s_waitcnt lgkmcnt(8)\n\t"
        "v_mfma_f32_16x16x32_bf16 v[116:119], v[52:55], v[64:67], v[116:119]\n\t"
        "s_waitcnt vmcnt(0) lgkmcnt(0)\n\t"
        "s_barrier\n\t"
        "s_cbranch_vccnz 611f\n\t"
        "612:\n\t"
        "v_mfma_f32_16x16x32_bf16 v[128:131], v[52:55], v[68:71], v[128:131]\n\t"
        "ds_read_b128 v[56:59], v24 offset:0\n\t"
        "v_mfma_f32_16x16x32_bf16 v[124:127], v[48:51], v[68:71], v[124:127]\n\t"
        "ds_read_b128 v[60:63], v25 offset:0\n\t"
        "v_mfma_f32_16x16x32_bf16 v[120:123], v[44:47], v[68:71], v[120:123]\n\t"
        "ds_read_b128 v[64:67], v26 offset:0\n\t"
        "v_mfma_f32_16x16x32_bf16 v[132:135], v[44:47], v[72:75], v[132:135]\n\t"
        "ds_read_b128 v[68:71], v27 offset:0\n\t"
        "v_mfma_f32_16x16x32_bf16 v[136:139], v[48:51], v[72:75], v[136:139]\n\t"
        "v_mfma_f32_16x16x32_bf16 v[140:143], v[52:55], v[72:75], v[140:143]\n\t"
        "ds_read_b128 v[72:75], v28 offset:0\n\t"
        "v_mfma_f32_16x16x32_bf16 v[152:155], v[52:55], v[76:79], v[152:155]\n\t"
        "v_mfma_f32_16x16x32_bf16 v[148:151], v[48:51], v[76:79], v[148:151]\n\t"
        "v_mfma_f32_16x16x32_bf16 v[144:147], v[44:47], v[76:79], v[144:147]\n\t"
        "ds_read_b128 v[76:79], v29 offset:0\n\t"
        "s_waitcnt lgkmcnt(5)\n\t"
        "s_branch 60b\n\t"
        "601:\n\t"
        "s_barrier\n\t"
        "s_branch 602b\n\t"
        "611:\n\t"
        "s_barrier\n\t"
        "s_branch 612b\n\t"
        "55:\n\t"
        "s_setprio 2\n\t"
        "s_waitcnt lgkmcnt(0)\n\t"
        ".p2align 6\n\t"
        "50:\n\t"
        "v_mfma_f32_16x16x32_bf16 v[84:87], v[32:35], v[56:59], v[84:87]\n\t"
        "ds_read_b64_tr_b16 v[44:45], %[wa0] offset:18432\n\t"
        "v_mfma_f32_16x16x32_bf16 v[88:91], v[36:39], v[56:59], v[88:91]\n\t"
        "ds_read_b64_tr_b16 v[46:47], %[wa0] offset:19200\n\t"
        "v_mfma_f32_16x16x32_bf16 v[92:95], v[40:43], v[56:59], v[92:95]\n\t"
        "ds_read_b128 v[56:59], v24 offset:64\n\t"
        "s_waitcnt lgkmcnt(8)\n\t"
        "v_mfma_f32_16x16x32_bf16 v[104:107], v[40:43], v[60:63], v[104:107]\n\t"
        "ds_read_b64_tr_b16 v[48:49], %[wa1] offset:18432\n\t"
        "v_mfma_f32_16x16x32_bf16 v[100:103], v[36:39], v[60:63], v[100:103]\n\t"
        "ds_read_b64_tr_b16 v[50:51], %[wa1] offset:19200\n\t"
        "v_mfma_f32_16x16x32_bf16 v[96:99], v[32:35], v[60:63], v[96:99]\n\t"
        "ds_read_b128 v[60:63], v25 offset:64\n\t"
        "s_waitcnt lgkmcnt(10)\n\t"
        "v_mfma_f32_16x16x32_bf16 v[108:111], v[32:35], v[64:67], v[108:111]\n\t"
        "ds_read_b64_tr_b16 v[52:53], %[wa2] offset:18432\n\t"
        "v_mfma_f32_16x16x32_bf16 v[112:115], v[36:39], v[64:67], v[112:115]\n\t"
        "ds_read_b64_tr_b16 v[54:55], %[wa2] offset:19200\n\t"
        "v_mfma_f32_16x16x32_bf16 v[116:119], v[40:43], v[64:67], v[116:119]\n\t"
        "ds_read_b128 v[64:67], v26 offset:64\n\t"
        "s_waitcnt lgkmcnt(12)\n\t"
        "v_mfma_f32_16x16x32_bf16 v[128:131], v[40:43], v[68:71], v[128:131]\n\t"
        "ds_read2_b32 v[14:15], %[ca] offset0:0 offset1:1\n\t"
        "v_mfma_f32_16x16x32_bf16 v[124:127], v[36:39], v[68:71], v[124:127]\n\t"
        "ds_read_b128 v[16:19], %[ta] offset:1024\n\t"
        "v_mfma_f32_16x16x32_bf16 v[120:123], v[32:35], v[68:71], v[120:123]\n\t"
        "ds_read_b128 v[68:71], v27 offset:64\n\t"
        "s_waitcnt lgkmcnt(14)\n\t"
        "v_mfma_f32_16x16x32_bf16 v[132:135], v[32:35], v[72:75], v[132:135]\n\t"
        "ds_read_b128 v[20:23], %[ta] offset:1280\n\t"
        "v_mfma_f32_16x16x32_bf16 v[136:139], v[36:39], v[72:75], v[136:139]\n\t"
        "v_mfma_f32_16x16x32_bf16 v[140:143], v[40:43], v[72:75], v[140:143]\n\t"
        "ds_read_b128 v[72:75], v28 offset:64\n\t"
        "s_waitcnt lgkmcnt(14)\n\t"
        "v_mfma_f32_16x16x32_bf16 v[152:155], v[40:43], v[76:79], v[152:155]\n\t"
        "v_mfma_f32_16x16x32_bf16 v[148:151], v[36:39], v[76:79], v[148:151]\n\t"
        "v_mfma_f32_16x16x32_bf16 v[144:147], v[32:35], v[76:79], v[144:147]\n\t"
        "ds_read_b128 v[76:79], v29 offset:64\n\t"
        "v_mfma_f32_16x16x32_bf16 v[156:159], v[32:35], v[80:83], v[156:159]\n\t"
        "s_waitcnt lgkmcnt(7)\n\t"
        "v_mfma_f32_16x16x32_bf16 v[160:163], v[36:39], v[80:83], v[160:163]\n\t"
        "ds_read_b128 v[80:83], v30 offset:64\n\t"
        "s_waitcnt lgkmcnt(3)\n\t"
        "v_mfma_f32_16x16x32_bf16 v[84:87], v[44:47], v[56:59], v[84:87]\n\t"
        "ds_read_b64_tr_b16 v[32:33], %[wa0] offset:24576\n\t"
        "v_add_u32 v14, v14, v15\n\t"
        "v_mfma_f32_16x16x32_bf16 v[88:91], v[48:51], v[56:59], v[88:91]\n\t"
        "ds_read_b64_tr_b16 v[34:35], %[wa0] offset:25344\n\t"
        "v_cmp_lt_u16 vcc, 208, v14\n\t"
        "v_mfma_f32_16x16x32_bf16 v[92:95], v[52:55], v[56:59], v[92:95]\n\t"
        "ds_read_b128 v[56:59], v24 offset:128\n\t"
        "v_mfma_f32_16x16x32_bf16 v[104:107], v[52:55], v[60:63], v[104:107]\n\t"
        "ds_read_b64_tr_b16 v[36:37], %[wa1] offset:24576\n\t"
        "v_xor_b32 v24, %[q16], v16\n\t"
        "v_mfma_f32_16x16x32_bf16 v[100:103], v[48:51], v[60:63], v[100:103]\n\t"
        "ds_read_b64_tr_b16 v[38:39], %[wa1] offset:25344\n\t"
        "v_mfma_f32_16x16x32_bf16 v[96:99], v[44:47], v[60:63], v[96:99]\n\t"
        "ds_read_b128 v[60:63], v25 offset:128\n\t"
        "v_mfma_f32_16x16x32_bf16 v[108:111], v[44:47], v[64:67], v[108:111]\n\t"
        "ds_read_b64_tr_b16 v[40:41], %[wa2] offset:24576\n\t"
        "v_xor_b32 v25, %[q16], v17\n\t"
        "v_mfma_f32_16x16x32_bf16 v[112:115], v[48:51], v[64:67], v[112:115]\n\t"
        "ds_read_b64_tr_b16 v[42:43], %[wa2] offset:25344\n\t"
        "v_mfma_f32_16x16x32_bf16 v[116:119], v[52:55], v[64:67], v[116:119]\n\t"
        "ds_read_b128 v[64:67], v26 offset:128\n\t"
        "v_mfma_f32_16x16x32_bf16 v[128:131], v[52:55], v[68:71], v[128:131]\n\t"
        "v_xor_b32 v26, %[q16], v18\n\t"
        "v_mfma_f32_16x16x32_bf16 v[124:127], v[48:51], v[68:71], v[124:127]\n\t"
        "s_waitcnt lgkmcnt(11)\n\t"
        "v_mfma_f32_16x16x32_bf16 v[120:123], v[44:47], v[68:71], v[120:123]\n\t"
        "ds_read_b128 v[68:71], v27 offset:128\n\t"
        "v_mfma_f32_16x16x32_bf16 v[132:135], v[44:47], v[72:75], v[132:135]\n\t"
        "v_xor_b32 v27, %[q16], v19\n\t"
        "v_mfma_f32_16x16x32_bf16 v[136:139], v[48:51], v[72:75], v[136:139]\n\t"
        "s_waitcnt lgkmcnt(11)\n\t"
        "v_mfma_f32_16x16x32_bf16 v[140:143], v[52:55], v[72:75], v[140:143]\n\t"
        "ds_read_b128 v[72:75], v28 offset:128\n\t"
        "v_mfma_f32_16x16x32_bf16 v[152:155], v[52:55], v[76:79], v[152:155]\n\t"
        "v_xor_b32 v28, %[q16], v20\n\t"
        "v_mfma_f32_16x16x32_bf16 v[148:151], v[48:51], v[76:79], v[148:151]\n\t"
        "s_waitcnt lgkmcnt(11)\n\t"
        "v_mfma_f32_16x16x32_bf16 v[144:147], v[44:47], v[76:79], v[144:147]\n\t"
        "ds_read_b128 v[76:79], v29 offset:128\n\t"
        "v_mfma_f32_16x16x32_bf16 v[156:159], v[44:47], v[80:83], v[156:159]\n\t"
        "v_xor_b32 v29, %[q16], v21\n\t"
        "s_waitcnt lgkmcnt(4)\n\t"
        "v_mfma_f32_16x16x32_bf16 v[160:163], v[48:51], v[80:83], v[160:163]\n\t"
        "ds_read_b128 v[80:83], v30 offset:128\n\t"
        "v_mfma_f32_16x16x32_bf16 v[84:87], v[32:35], v[56:59], v[84:87]\n\t"
        "ds_read_b64_tr_b16 v[44:45], %[wa0] offset:30720\n\t"
        "v_xor_b32 v30, %[q16], v22\n\t"
        "v_mfma_f32_16x16x32_bf16 v[88:91], v[36:39], v[56:59], v[88:91]\n\t"
        "ds_read_b64_tr_b16 v[46:47], %[wa0] offset:31488\n\t"
        "v_mfma_f32_16x16x32_bf16 v[92:95], v[40:43], v[56:59], v[92:95]\n\t"
        "ds_read_b64_tr_b16 v[48:49], %[wa1] offset:30720\n\t"
        "v_mfma_f32_16x16x32_bf16 v[104:107], v[40:43], v[60:63], v[104:107]\n\t"
        "ds_read_b64_tr_b16 v[50:51], %[wa1] offset:31488\n\t"
        "v_mfma_f32_16x16x32_bf16 v[100:103], v[36:39], v[60:63], v[100:103]\n\t"
        "ds_read_b64_tr_b16 v[52:53], %[wa2] offset:30720\n\t"
        "s_waitcnt lgkmcnt(9)\n\t"
        "v_mfma_f32_16x16x32_bf16 v[96:99], v[32:35], v[60:63], v[96:99]\n\t"
        "ds_read_b64_tr_b16 v[54:55], %[wa2] offset:31488\n\t"
        "v_mfma_f32_16x16x32_bf16 v[108:111], v[32:35], v[64:67], v[108:111]\n\t"
        "v_mfma_f32_16x16x32_bf16 v[112:115], v[36:39], v[64:67], v[112:115]\n\t"
        "s_waitcnt lgkmcnt(9)\n\t"
        "v_mfma_f32_16x16x32_bf16 v[116:119], v[40:43], v[64:67], v[116:119]\n\t"
        "s_waitcnt lgkmcnt(0)\n\t"
        "s_barrier\n\t"
        "s_cbranch_vccnz 501f\n\t"
        "502:\n\t"
        "v_mfma_f32_16x16x32_bf16 v[128:131], v[40:43], v[68:71], v[128:131]\n\t"
        "ds_read_b128 v[56:59], v24 offset:0\n\t"
        "v_mfma_f32_16x16x32_bf16 v[124:127], v[36:39], v[68:71], v[124:127]\n\t"
        "ds_read_b128 v[60:63], v25 offset:0\n\t"
        "v_mfma_f32_16x16x32_bf16 v[120:123], v[32:35], v[68:71], v[120:123]\n\t"
        "ds_read_b128 v[64:67], v26 offset:0\n\t"
        "v_mfma_f32_16x16x32_bf16 v[132:135], v[32:35], v[72:75], v[132:135]\n\t"
        "ds_read_b128 v[68:71], v27 offset:0\n\t"
        "v_mfma_f32_16x16x32_bf16 v[136:139], v[36:39], v[72:75], v[136:139]\n\t"
        "v_mfma_f32_16x16x32_bf16 v[140:143], v[40:43], v[72:75], v[140:143]\n\t"
        "ds_read_b128 v[72:75], v28 offset:0\n\t"
        "v_mfma_f32_16x16x32_bf16 v[152:155], v[40:43], v[76:79], v[152:155]\n\t"
        "v_mfma_f32_16x16x32_bf16 v[148:151], v[36:39], v[76:79], v[148:151]\n\t"
        "v_mfma_f32_16x16x32_bf16 v[144:147], v[32:35], v[76:79], v[144:147]\n\t"
        "ds_read_b128 v[76:79], v29 offset:0\n\t"
        "v_mfma_f32_16x16x32_bf16 v[156:159], v[32:35], v[80:83], v[156:159]\n\t"
        "s_sub_u32 %[tn], %[tn], 1\n\t"
        "v_mfma_f32_16x16x32_bf16 v[160:163], v[36:39], v[80:83], v[160:163]\n\t"
        "ds_read_b128 v[80:83], v30 offset:0\n\t"
        "s_cbranch_scc1 9f\n\t"
        "s_waitcnt lgkmcnt(6)\n\t"
        "v_mfma_f32_16x16x32_bf16 v[84:87], v[44:47], v[56:59], v[84:87]\n\t"
        "ds_read_b64_tr_b16 v[32:33], %[wa0] offset:0\n\t"
        "v_mfma_f32_16x16x32_bf16 v[88:91], v[48:51], v[56:59], v[88:91]\n\t"
        "ds_read_b64_tr_b16 v[34:35], %[wa0] offset:768\n\t"
        "v_mfma_f32_16x16x32_bf16 v[92:95], v[52:55], v[56:59], v[92:95]\n\t"
        "ds_read_b128 v[56:59], v24 offset:64\n\t"
        "s_waitcnt lgkmcnt(8)\n\t"
        "v_mfma_f32_16x16x32_bf16 v[104:107], v[52:55], v[60:63], v[104:107]\n\t"
        "ds_read_b64_tr_b16 v[36:37], %[wa1] offset:0\n\t"
        "v_mfma_f32_16x16x32_bf16 v[100:103], v[48:51], v[60:63], v[100:103]\n\t"
        "ds_read_b64_tr_b16 v[38:39], %[wa1] offset:768\n\t"
        "v_mfma_f32_16x16x32_bf16 v[96:99], v[44:47], v[60:63], v[96:99]\n\t"
        "ds_read_b128 v[60:63], v25 offset:64\n\t"
        "s_waitcnt lgkmcnt(10)\n\t"
        "v_mfma_f32_16x16x32_bf16 v[108:111], v[44:47], v[64:67], v[108:111]\n\t"
        "ds_read_b64_tr_b16 v[40:41], %[wa2] offset:0\n\t"
        "v_mfma_f32_16x16x32_bf16 v[112:115], v[48:51], v[64:67], v[112:115]\n\t"
        "ds_read_b64_tr_b16 v[42:43], %[wa2] offset:768\n\t"
        "v_mfma_f32_16x16x32_bf16 v[116:119], v[52:55], v[64:67], v[116:119]\n\t"
        "ds_read_b128 v[64:67], v26 offset:64\n\t"
        "s_waitcnt lgkmcnt(12)\n\t"
        "v_mfma_f32_16x16x32_bf16 v[128:131], v[52:55], v[68:71], v[128:131]\n\t"
        "ds_read2_b32 v[14:15], %[ca] offset0:1 offset1:2\n\t"
        "v_mfma_f32_16x16x32_bf16 v[124:127], v[48:51], v[68:71], v[124:127]\n\t"
        "ds_read_b128 v[16:19], %[ta] offset:0\n\t"
        "v_mfma_f32_16x16x32_bf16 v[120:123], v[44:47], v[68:71], v[120:123]\n\t"
        "ds_read_b128 v[68:71], v27 offset:64\n\t"
        "s_waitcnt lgkmcnt(14)\n\t"
        "v_mfma_f32_16x16x32_bf16 v[132:135], v[44:47], v[72:75], v[132:135]\n\t"
        "ds_read_b128 v[20:23], %[ta] offset:256\n\t"
        "v_mfma_f32_16x16x32_bf16 v[136:139], v[48:51], v[72:75], v[136:139]\n\t"
        "v_mfma_f32_16x16x32_bf16 v[140:143], v[52:55], v[72:75], v[140:143]\n\t"
        "ds_read_b128 v[72:75], v28 offset:64\n\t"
        "s_waitcnt lgkmcnt(14)\n\t"
        "v_mfma_f32_16x16x32_bf16 v[152:155], v[52:55], v[76:79], v[152:155]\n\t"
        "v_mfma_f32_16x16x32_bf16 v[148:151], v[48:51], v[76:79], v[148:151]\n\t"
        "v_mfma_f32_16x16x32_bf16 v[144:147], v[44:47], v[76:79], v[144:147]\n\t"
        "ds_read_b128 v[76:79], v29 offset:64\n\t"
        "v_mfma_f32_16x16x32_bf16 v[156:159], v[44:47], v[80:83], v[156:159]\n\t"
        "s_waitcnt lgkmcnt(7)\n\t"
        "v_mfma_f32_16x16x32_bf16 v[160:163], v[48:51], v[80:83], v[160:163]\n\t"
        "ds_read_b128 v[80:83], v30 offset:64\n\t"
        "s_waitcnt lgkmcnt(3)\n\t"
        "v_mfma_f32_16x16x32_bf16 v[84:87], v[32:35], v[56:59], v[84:87]\n\t"
        "ds_read_b64_tr_b16 v[44:45], %[wa0] offset:6144\n\t"
        "v_add_u32 v14, v14, v15\n\t"
        "v_mfma_f32_16x16x32_bf16 v[88:91], v[36:39], v[56:59], v[88:91]\n\t"
        "ds_read_b64_tr_b16 v[46:47], %[wa0] offset:6912\n\t"
        "v_cmp_lt_u16 vcc, 208, v14\n\t"
        "v_mfma_f32_16x16x32_bf16 v[92:95], v[40:43], v[56:59], v[92:95]\n\t"
        "ds_read_b128 v[56:59], v24 offset:128\n\t"
        "v_add_u32 %[ca], 8, %[ca]\n\t"
        "v_mfma_f32_16x16x32_bf16 v[104:107], v[40:43], v[60:63], v[104:107]\n\t"
        "ds_read_b64_tr_b16 v[48:49], %[wa1] offset:6144\n\t"
        "v_xor_b32 v24, %[q16], v16\n\t"
        "v_mfma_f32_16x16x32_bf16 v[100:103], v[36:39], v[60:63], v[100:103]\n\t"
        "ds_read_b64_tr_b16 v[50:51], %[wa1] offset:6912\n\t"
        "v_mfma_f32_16x16x32_bf16 v[96:99], v[32:35], v[60:63], v[96:99]\n\t"
        "ds_read_b128 v[60:63], v25 offset:128\n\t"
        "v_mfma_f32_16x16x32_bf16 v[108:111], v[32:35], v[64:67], v[108:111]\n\t"
        "ds_read_b64_tr_b16 v[52:53], %[wa2] offset:6144\n\t"
        "v_xor_b32 v25, %[q16], v17\n\t"
        "v_mfma_f32_16x16x32_bf16 v[112:115], v[36:39], v[64:67], v[112:115]\n\t"
        "ds_read_b64_tr_b16 v[54:55], %[wa2] offset:6912\n\t"
        "v_mfma_f32_16x16x32_bf16 v[116:119], v[40:43], v[64:67], v[116:119]\n\t"
        "ds_read_b128 v[64:67], v26 offset:128\n\t"
        "v_mfma_f32_16x16x32_bf16 v[128:131], v[40:43], v[68:71], v[128:131]\n\t"
        "v_xor_b32 v26, %[q16], v18\n\t"
        "v_mfma_f32_16x16x32_bf16 v[124:127], v[36:39], v[68:71], v[124:127]\n\t"
        "s_waitcnt lgkmcnt(11)\n\t"
        "v_mfma_f32_16x16x32_bf16 v[120:123], v[32:35], v[68:71], v[120:123]\n\t"
        "ds_read_b128 v[68:71], v27 offset:128\n\t"
        "v_mfma_f32_16x16x32_bf16 v[132:135], v[32:35], v[72:75], v[132:135]\n\t"
        "v_xor_b32 v27, %[q16], v19\n\t"
        "v_mfma_f32_16x16x32_bf16 v[136:139], v[36:39], v[72:75], v[136:139]\n\t"
        "s_waitcnt lgkmcnt(11)\n\t"
        "v_mfma_f32_16x16x32_bf16 v[140:143], v[40:43], v[72:75], v[140:143]\n\t"
        "ds_read_b128 v[72:75], v28 offset:128\n\t"
        "v_mfma_f32_16x16x32_bf16 v[152:155], v[40:43], v[76:79], v[152:155]\n\t"
        "v_xor_b32 v28, %[q16], v20\n\t"
        "v_mfma_f32_16x16x32_bf16 v[148:151], v[36:39], v[76:79], v[148:151]\n\t"
        "s_waitcnt lgkmcnt(11)\n\t"
        "v_mfma_f32_16x16x32_bf16 v[144:147], v[32:35], v[76:79], v[144:147]\n\t"
        "ds_read_b128 v[76:79], v29 offset:128\n\t"
        "v_mfma_f32_16x16x32_bf16 v[156:159], v[32:35], v[80:83], v[156:159]\n\t"
        "v_xor_b32 v29, %[q16], v21\n\t"
        "s_waitcnt lgkmcnt(4)\n\t"
        "v_mfma_f32_16x16x32_bf16 v[160:163], v[36:39], v[80:83], v[160:163]\n\t"
        "ds_read_b128 v[80:83], v30 offset:128\n\t"
        "v_mfma_f32_16x16x32_bf16 v[84:87], v[44:47], v[56:59], v[84:87]\n\t"
        "ds_read_b64_tr_b16 v[32:33], %[wa0] offset:12288\n\t"
        "v_xor_b32 v30, %[q16], v22\n\t"
        "v_mfma_f32_16x16x32_bf16 v[88:91], v[48:51], v[56:59], v[88:91]\n\t"
        "ds_read_b64_tr_b16 v[34:35], %[wa0] offset:13056\n\t"
        "v_mfma_f32_16x16x32_bf16 v[92:95], v[52:55], v[56:59], v[92:95]\n\t"
        "ds_read_b64_tr_b16 v[36:37], %[wa1] offset:12288\n\t"
        "v_mfma_f32_16x16x32_bf16 v[104:107], v[52:55], v[60:63], v[104:107]\n\t"
        "ds_read_b64_tr_b16 v[38:39], %[wa1] offset:13056\n\t"
        "v_mfma_f32_16x16x32_bf16 v[100:103], v[48:51], v[60:63], v[100:103]\n\t"
        "ds_read_b64_tr_b16 v[40:41], %[wa2] offset:12288\n\t"
        "s_waitcnt lgkmcnt(9)\n\t"
        "v_mfma_f32_16x16x32_bf16 v[96:99], v[44:47], v[60:63], v[96:99]\n\t"
        "ds_read_b64_tr_b16 v[42:43], %[wa2] offset:13056\n\t"
        "v_mfma_f32_16x16x32_bf16 v[108:111], v[44:47], v[64:67], v[108:111]\n\t"
        "v_mfma_f32_16x16x32_bf16 v[112:115], v[48:51], v[64:67], v[112:115]\n\t"
        "s_waitcnt lgkmcnt(9)\n\t"
        "v_mfma_f32_16x16x32_bf16 v[116:119], v[52:55], v[64:67], v[116:119]\n\t"
        "s_waitcnt vmcnt(0) lgkmcnt(0)\n\t"
        "s_barrier\n\t"
        "s_cbranch_vccnz 511f\n\t"
        "512:\n\t"
        "v_mfma_f32_16x16x32_bf16 v[128:131], v[52:55], v[68:71], v[128:131]\n\t"
        "ds_read_b128 v[56:59], v24 offset:0\n\t"
        "v_mfma_f32_16x16x32_bf16 v[124:127], v[48:51], v[68:71], v[124:127]\n\t"
        "ds_read_b128 v[60:63], v25 offset:0\n\t"
        "v_mfma_f32_16x16x32_bf16 v[120:123], v[44:47], v[68:71], v[120:123]\n\t"
        "ds_read_b128 v[64:67], v26 offset:0\n\t"
        "v_mfma_f32_16x16x32_bf16 v[132:135], v[44:47], v[72:75], v[132:135]\n\t"
        "ds_read_b128 v[68:71], v27 offset:0\n\t"
        "v_mfma_f32_16x16x32_bf16 v[136:139], v[48:51], v[72:75], v[136:139]\n\t"
        "v_mfma_f32_16x16x32_bf16 v[140:143], v[52:55], v[72:75], v[140:143]\n\t"
        "ds_read_b128 v[72:75], v28 offset:0\n\t"
        "v_mfma_f32_16x16x32_bf16 v[152:155], v[52:55], v[76:79], v[152:155]\n\t"
        "v_mfma_f32_16x16x32_bf16 v[148:151], v[48:51], v[76:79], v[148:151]\n\t"
        "v_mfma_f32_16x16x32_bf16 v[144:147], v[44:47], v[76:79], v[144:147]\n\t"
        "ds_read_b128 v[76:79], v29 offset:0\n\t"
        "v_mfma_f32_16x16x32_bf16 v[156:159], v[44:47], v[80:83], v[156:159]\n\t"
        "v_mfma_f32_16x16x32_bf16 v[160:163], v[48:51], v[80:83], v[160:163]\n\t"
        "ds_read_b128 v[80:83], v30 offset:0\n\t"
        "s_waitcnt lgkmcnt(6)\n\t"
        "s_branch 50b\n\t"
        "501:\n\t"
        "s_barrier\n\t"
        "s_branch 502b\n\t"
        "511:\n\t"
        "s_barrier\n\t"
        "s_branch 512b\n\t"
        "45:\n\t"
        "s_setprio 0\n\t"
        "s_waitcnt lgkmcnt(0)\n\t"
        ".p2align 6\n\t"
        "40:\n\t"
        "v_mfma_f32_16x16x32_bf16 v[84:87], v[32:35], v[56:59], v[84:87]\n\t"
        "ds_read_b64_tr_b16 v[44:45], %[wa0] offset:18432\n\t"
        "v_mfma_f32_16x16x32_bf16 v[88:91], v[36:39], v[56:59], v[88:91]\n\t"
        "ds_read_b64_tr_b16 v[46:47], %[wa0] offset:19200\n\t"
        "v_mfma_f32_16x16x32_bf16 v[92:95], v[40:43], v[56:59], v[92:95]\n\t"
        "ds_read_b128 v[56:59], v24 offset:64\n\t"
        "s_waitcnt lgkmcnt(8)\n\t"
        "v_mfma_f32_16x16x32_bf16 v[104:107], v[40:43], v[60:63], v[104:107]\n\t"
        "ds_read_b64_tr_b16 v[48:49], %[wa1] offset:18432\n\t"
        "v_mfma_f32_16x16x32_bf16 v[100:103], v[36:39], v[60:63], v[100:103]\n\t"
        "ds_read_b64_tr_b16 v[50:51], %[wa1] offset:19200\n\t"
        "v_mfma_f32_16x16x32_bf16 v[96:99], v[32:35], v[60:63], v[96:99]\n\t"
        "ds_read_b128 v[60:63], v25 offset:64\n\t"
        "s_waitcnt lgkmcnt(10)\n\t"
        "v_mfma_f32_16x16x32_bf16 v[108:111], v[32:35], v[64:67], v[108:111]\n\t"
        "ds_read_b64_tr_b16 v[52:53], %[wa2] offset:18432\n\t"
        "v_mfma_f32_16x16x32_bf16 v[112:115], v[36:39], v[64:67], v[112:115]\n\t"
        "ds_read_b64_tr_b16 v[54:55], %[wa2] offset:19200\n\t"
        "v_mfma_f32_16x16x32_bf16 v[116:119], v[40:43], v[64:67], v[116:119]\n\t"
        "ds_read_b128 v[64:67], v26 offset:64\n\t"
        "s_waitcnt lgkmcnt(12)\n\t"
        "v_mfma_f32_16x16x32_bf16 v[128:131], v[40:43], v[68:71], v[128:131]\n\t"
        "ds_read2_b32 v[14:15], %[ca] offset0:0 offset1:1\n\t"
        "v_mfma_f32_16x16x32_bf16 v[124:127], v[36:39], v[68:71], v[124:127]\n\t"
        "ds_read_b128 v[16:19], %[ta] offset:1024\n\t"
        "v_mfma_f32_16x16x32_bf16 v[120:123], v[32:35], v[68:71], v[120:123]\n\t"
        "ds_read_b128 v[68:71], v27 offset:64\n\t"
        "s_waitcnt lgkmcnt(14)\n\t"
        "v_mfma_f32_16x16x32_bf16 v[132:135], v[32:35], v[72:75], v[132:135]\n\t"
        "ds_read_b128 v[20:23], %[ta] offset:1280\n\t"
        "v_mfma_f32_16x16x32_bf16 v[136:139], v[36:39], v[72:75], v[136:139]\n\t"
        "ds_read_b32 v22, %[ta] offset:1800\n\t"
        "v_mfma_f32_16x16x32_bf16 v[140:143], v[40:43], v[72:75], v[140:143]\n\t"
        "ds_read_b128 v[72:75], v28 offset:64\n\t"
        "s_waitcnt lgkmcnt(14)\n\t"
        "v_mfma_f32_16x16x32_bf16 v[152:155], v[40:43], v[76:79], v[152:155]\n\t"
        "v_mfma_f32_16x16x32_bf16 v[148:151], v[36:39], v[76:79], v[148:151]\n\t"
        "v_mfma_f32_16x16x32_bf16 v[144:147], v[32:35], v[76:79], v[144:147]\n\t"
        "ds_read_b128 v[76:79], v29 offset:64\n\t"
        "v_mfma_f32_16x16x32_bf16 v[164:167], v[40:43], v[80:83], v[164:167]\n\t"
        "ds_read_b128 v[80:83], v30 offset:64\n\t"
        "s_waitcnt lgkmcnt(9)\n\t"
        "v_mfma_f32_16x16x32_bf16 v[84:87], v[44:47], v[56:59], v[84:87]\n\t"
        "ds_read_b64_tr_b16 v[32:33], %[wa0] offset:24576\n\t"
        "v_mfma_f32_16x16x32_bf16 v[88:91], v[48:51], v[56:59], v[88:91]\n\t"
        "ds_read_b64_tr_b16 v[34:35], %[wa0] offset:25344\n\t"
        "s_waitcnt lgkmcnt(5)\n\t"
        "v_mfma_f32_16x16x32_bf16 v[92:95], v[52:55], v[56:59], v[92:95]\n\t"
        "ds_read_b128 v[56:59], v24 offset:128\n\t"
        "v_add_u32 v14, v14, v15\n\t"
        "v_mfma_f32_16x16x32_bf16 v[104:107], v[52:55], v[60:63], v[104:107]\n\t"
        "ds_read_b64_tr_b16 v[36:37], %[wa1] offset:24576\n\t"
        "v_cmp_lt_u16 vcc, 208, v14\n\t"
        "v_mfma_f32_16x16x32_bf16 v[100:103], v[48:51], v[60:63], v[100:103]\n\t"
        "ds_read_b64_tr_b16 v[38:39], %[wa1] offset:25344\n\t"
        "v_xor_b32 v24, %[q16], v16\n\t"
        "v_mfma_f32_16x16x32_bf16 v[96:99], v[44:47], v[60:63], v[96:99]\n\t"
        "ds_read_b128 v[60:63], v25 offset:128\n\t"
        "v_mfma_f32_16x16x32_bf16 v[108:111], v[44:47], v[64:67], v[108:111]\n\t"
        "ds_read_b64_tr_b16 v[40:41], %[wa2] offset:24576\n\t"
        "v_xor_b32 v25, %[q16], v17\n\t"
        "v_mfma_f32_16x16x32_bf16 v[112:115], v[48:51], v[64:67], v[112:115]\n\t"
        "ds_read_b64_tr_b16 v[42:43], %[wa2] offset:25344\n\t"
        "v_mfma_f32_16x16x32_bf16 v[116:119], v[52:55], v[64:67], v[116:119]\n\t"
        "ds_read_b128 v[64:67], v26 offset:128\n\t"
        "v_mfma_f32_16x16x32_bf16 v[128:131], v[52:55], v[68:71], v[128:131]\n\t"
        "v_xor_b32 v26, %[q16], v18\n\t"
        "v_mfma_f32_16x16x32_bf16 v[124:127], v[48:51], v[68:71], v[124:127]\n\t"
        "s_waitcnt lgkmcnt(11)\n\t"
        "v_mfma_f32_16x16x32_bf16 v[120:123], v[44:47], v[68:71], v[120:123]\n\t"
        "ds_read_b128 v[68:71], v27 offset:128\n\t"
        "v_mfma_f32_16x16x32_bf16 v[132:135], v[44:47], v[72:75], v[132:135]\n\t"
        "v_xor_b32 v27, %[q16], v19\n\t"
        "v_mfma_f32_16x16x32_bf16 v[136:139], v[48:51], v[72:75], v[136:139]\n\t"
        "s_waitcnt lgkmcnt(11)\n\t"
        "v_mfma_f32_16x16x32_bf16 v[140:143], v[52:55], v[72:75], v[140:143]\n\t"
        "ds_read_b128 v[72:75], v28 offset:128\n\t"
        "v_mfma_f32_16x16x32_bf16 v[152:155], v[52:55], v[76:79], v[152:155]\n\t"
        "v_xor_b32 v28, %[q16], v20\n\t"
        "v_mfma_f32_16x16x32_bf16 v[148:151], v[48:51], v[76:79], v[148:151]\n\t"
        "s_waitcnt lgkmcnt(11)\n\t"
        "v_mfma_f32_16x16x32_bf16 v[144:147], v[44:47], v[76:79], v[144:147]\n\t"
        "ds_read_b128 v[76:79], v29 offset:128\n\t"
        "s_waitcnt lgkmcnt(4)\n\t"
        "v_mfma_f32_16x16x32_bf16 v[164:167], v[52:55], v[80:83], v[164:167]\n\t"
        "ds_read_b128 v[80:83], v30 offset:128\n\t"
        "v_xor_b32 v29, %[q16], v21\n\t"
        "v_mfma_f32_16x16x32_bf16 v[84:87], v[32:35], v[56:59], v[84:87]\n\t"
        "ds_read_b64_tr_b16 v[44:45], %[wa0] offset:30720\n\t"
        "v_xor_b32 v30, %[q16], v22\n\t"
        "v_mfma_f32_16x16x32_bf16 v[88:91], v[36:39], v[56:59], v[88:91]\n\t"
        "ds_read_b64_tr_b16 v[46:47], %[wa0] offset:31488\n\t"
        "v_mfma_f32_16x16x32_bf16 v[92:95], v[40:43], v[56:59], v[92:95]\n\t"
        "ds_read_b64_tr_b16 v[48:49], %[wa1] offset:30720\n\t"
        "v_mfma_f32_16x16x32_bf16 v[104:107], v[40:43], v[60:63], v[104:107]\n\t"
        "ds_read_b64_tr_b16 v[50:51], %[wa1] offset:31488\n\t"
        "v_mfma_f32_16x16x32_bf16 v[100:103], v[36:39], v[60:63], v[100:103]\n\t"
        "ds_read_b64_tr_b16 v[52:53], %[wa2] offset:30720\n\t"
        "s_waitcnt lgkmcnt(9)\n\t"
        "v_mfma_f32_16x16x32_bf16 v[96:99], v[32:35], v[60:63], v[96:99]\n\t"
        "ds_read_b64_tr_b16 v[54:55], %[wa2] offset:31488\n\t"
        "v_mfma_f32_16x16x32_bf16 v[108:111], v[32:35], v[64:67], v[108:111]\n\t"
        "v_mfma_f32_16x16x32_bf16 v[112:115], v[36:39], v[64:67], v[112:115]\n\t"
        "s_waitcnt lgkmcnt(9)\n\t"
        "v_mfma_f32_16x16x32_bf16 v[116:119], v[40:43], v[64:67], v[116:119]\n\t"
        "s_waitcnt lgkmcnt(0)\n\t"
        "s_barrier\n\t"
        "s_cbranch_vccnz 401f\n\t"
        "402:\n\t"
        "v_mfma_f32_16x16x32_bf16 v[128:131], v[40:43], v[68:71], v[128:131]\n\t"
        "ds_read_b128 v[56:59], v24 offset:0\n\t"
        "v_mfma_f32_16x16x32_bf16 v[124:127], v[36:39], v[68:71], v[124:127]\n\t"
        "ds_read_b128 v[60:63], v25 offset:0\n\t"
        "v_mfma_f32_16x16x32_bf16 v[120:123], v[32:35], v[68:71], v[120:123]\n\t"
        "ds_read_b128 v[64:67], v26 offset:0\n\t"
        "v_mfma_f32_16x16x32_bf16 v[132:135], v[32:35], v[72:75], v[132:135]\n\t"
        "ds_read_b128 v[68:71], v27 offset:0\n\t"
        "v_mfma_f32_16x16x32_bf16 v[136:139], v[36:39], v[72:75], v[136:139]\n\t"
        "v_mfma_f32_16x16x32_bf16 v[140:143], v[40:43], v[72:75], v[140:143]\n\t"
        "ds_read_b128 v[72:75], v28 offset:0\n\t"
        "v_mfma_f32_16x16x32_bf16 v[152:155], v[40:43], v[76:79], v[152:155]\n\t"
        "v_mfma_f32_16x16x32_bf16 v[148:151], v[36:39], v[76:79], v[148:151]\n\t"
        "v_mfma_f32_16x16x32_bf16 v[144:147], v[32:35], v[76:79], v[144:147]\n\t"
        "ds_read_b128 v[76:79], v29 offset:0\n\t"
        "s_sub_u32 %[tn], %[tn], 1\n\t"
        "v_mfma_f32_16x16x32_bf16 v[164:167], v[40:43], v[80:83], v[164:167]\n\t"
        "ds_read_b128 v[80:83], v30 offset:0\n\t"
        "s_cbranch_scc1 9f\n\t"
        "s_waitcnt lgkmcnt(6)\n\t"
        "v_mfma_f32_16x16x32_bf16 v[84:87], v[44:47], v[56:59], v[84:87]\n\t"
        "ds_read_b64_tr_b16 v[32:33], %[wa0] offset:0\n\t"
        "v_mfma_f32_16x16x32_bf16 v[88:91], v[48:51], v[56:59], v[88:91]\n\t"
        "ds_read_b64_tr_b16 v[34:35], %[wa0] offset:768\n\t"
        "v_mfma_f32_16x16x32_bf16 v[92:95], v[52:55], v[56:59], v[92:95]\n\t"
        "ds_read_b128 v[56:59], v24 offset:64\n\t"
        "s_waitcnt lgkmcnt(8)\n\t"
        "v_mfma_f32_16x16x32_bf16 v[104:107], v[52:55], v[60:63], v[104:107]\n\t"
        "ds_read_b64_tr_b16 v[36:37], %[wa1] offset:0\n\t"
        "v_mfma_f32_16x16x32_bf16 v[100:103], v[48:51], v[60:63], v[100:103]\n\t"
        "ds_read_b64_tr_b16 v[38:39], %[wa1] offset:768\n\t"
        "v_mfma_f32_16x16x32_bf16 v[96:99], v[44:47], v[60:63], v[96:99]\n\t"
        "ds_read_b128 v[60:63], v25 offset:64\n\t"
        "s_waitcnt lgkmcnt(10)\n\t"
        "v_mfma_f32_16x16x32_bf16 v[108:111], v[44:47], v[64:67], v[108:111]\n\t"
        "ds_read_b64_tr_b16 v[40:41], %[wa2] offset:0\n\t"
        "v_mfma_f32_16x16x32_bf16 v[112:115], v[48:51], v[64:67], v[112:115]\n\t"
        "ds_read_b64_tr_b16 v[42:43], %[wa2] offset:768\n\t"
        "v_mfma_f32_16x16x32_bf16 v[116:119], v[52:55], v[64:67], v[116:119]\n\t"
        "ds_read_b128 v[64:67], v26 offset:64\n\t"
        "s_waitcnt lgkmcnt(12)\n\t"
        "v_mfma_f32_16x16x32_bf16 v[128:131], v[52:55], v[68:71], v[128:131]\n\t"
        "ds_read2_b32 v[14:15], %[ca] offset0:1 offset1:2\n\t"
        "v_mfma_f32_16x16x32_bf16 v[124:127], v[48:51], v[68:71], v[124:127]\n\t"
        "ds_read_b128 v[16:19], %[ta] offset:0\n\t"
        "v_mfma_f32_16x16x32_bf16 v[120:123], v[44:47], v[68:71], v[120:123]\n\t"
        "ds_read_b128 v[68:71], v27 offset:64\n\t"
        "s_waitcnt lgkmcnt(14)\n\t"
        "v_mfma_f32_16x16x32_bf16 v[132:135], v[44:47], v[72:75], v[132:135]\n\t"
        "ds_read_b128 v[20:23], %[ta] offset:256\n\t"
        "v_mfma_f32_16x16x32_bf16 v[136:139], v[48:51], v[72:75], v[136:139]\n\t"
        "ds_read_b32 v22, %[ta] offset:776\n\t"
        "v_mfma_f32_16x16x32_bf16 v[140:143], v[52:55], v[72:75], v[140:143]\n\t"
        "ds_read_b128 v[72:75], v28 offset:64\n\t"
        "s_waitcnt lgkmcnt(14)\n\t"
        "v_mfma_f32_16x16x32_bf16 v[152:155], v[52:55], v[76:79], v[152:155]\n\t"
        "v_mfma_f32_16x16x32_bf16 v[148:151], v[48:51], v[76:79], v[148:151]\n\t"
        "v_mfma_f32_16x16x32_bf16 v[144:147], v[44:47], v[76:79], v[144:147]\n\t"
        "ds_read_b128 v[76:79], v29 offset:64\n\t"
        "v_mfma_f32_16x16x32_bf16 v[164:167], v[52:55], v[80:83], v[164:167]\n\t"
        "ds_read_b128 v[80:83], v30 offset:64\n\t"
        "s_waitcnt lgkmcnt(9)\n\t"
        "v_mfma_f32_16x16x32_bf16 v[84:87], v[32:35], v[56:59], v[84:87]\n\t"
        "ds_read_b64_tr_b16 v[44:45], %[wa0] offset:6144\n\t"
        "v_mfma_f32_16x16x32_bf16 v[88:91], v[36:39], v[56:59], v[88:91]\n\t"
        "ds_read_b64_tr_b16 v[46:47], %[wa0] offset:6912\n\t"
        "s_waitcnt lgkmcnt(5)\n\t"
        "v_mfma_f32_16x16x32_bf16 v[92:95], v[40:43], v[56:59], v[92:95]\n\t"
        "ds_read_b128 v[56:59], v24 offset:128\n\t"
        "v_add_u32 v14, v14, v15\n\t"
        "v_mfma_f32_16x16x32_bf16 v[104:107], v[40:43], v[60:63], v[104:107]\n\t"
        "ds_read_b64_tr_b16 v[48:49], %[wa1] offset:6144\n\t"
        "v_cmp_lt_u16 vcc, 208, v14\n\t"
        "v_mfma_f32_16x16x32_bf16 v[100:103], v[36:39], v[60:63], v[100:103]\n\t"
        "ds_read_b64_tr_b16 v[50:51], %[wa1] offset:6912\n\t"
        "v_add_u32 %[ca], 8, %[ca]\n\t"
        "v_mfma_f32_16x16x32_bf16 v[96:99], v[32:35], v[60:63], v[96:99]\n\t"
        "ds_read_b128 v[60:63], v25 offset:128\n\t"
        "v_xor_b32 v24, %[q16], v16\n\t"
        "v_mfma_f32_16x16x32_bf16 v[108:111], v[32:35], v[64:67], v[108:111]\n\t"
        "ds_read_b64_tr_b16 v[52:53], %[wa2] offset:6144\n\t"
        "v_xor_b32 v25, %[q16], v17\n\t"
        "v_mfma_f32_16x16x32_bf16 v[112:115], v[36:39], v[64:67], v[112:115]\n\t"
        "ds_read_b64_tr_b16 v[54:55], %[wa2] offset:6912\n\t"
        "v_mfma_f32_16x16x32_bf16 v[116:119], v[40:43], v[64:67], v[116:119]\n\t"
        "ds_read_b128 v[64:67], v26 offset:128\n\t"
        "v_mfma_f32_16x16x32_bf16 v[128:131], v[40:43], v[68:71], v[128:131]\n\t"
        "v_xor_b32 v26, %[q16], v18\n\t"
        "v_mfma_f32_16x16x32_bf16 v[124:127], v[36:39], v[68:71], v[124:127]\n\t"
        "s_waitcnt lgkmcnt(11)\n\t"
        "v_mfma_f32_16x16x32_bf16 v[120:123], v[32:35], v[68:71], v[120:123]\n\t"
        "ds_read_b128 v[68:71], v27 offset:128\n\t"
        "v_mfma_f32_16x16x32_bf16 v[132:135], v[32:35], v[72:75], v[132:135]\n\t"
        "v_xor_b32 v27, %[q16], v19\n\t"
        "v_mfma_f32_16x16x32_bf16 v[136:139], v[36:39], v[72:75], v[136:139]\n\t"
        "s_waitcnt lgkmcnt(11)\n\t"
        "v_mfma_f32_16x16x32_bf16 v[140:143], v[40:43], v[72:75], v[140:143]\n\t"
        "ds_read_b128 v[72:75], v28 offset:128\n\t"
        "v_mfma_f32_16x16x32_bf16 v[152:155], v[40:43], v[76:79], v[152:155]\n\t"
        "v_xor_b32 v28, %[q16], v20\n\t"
        "v_mfma_f32_16x16x32_bf16 v[148:151], v[36:39], v[76:79], v[148:151]\n\t"
        "s_waitcnt lgkmcnt(11)\n\t"
        "v_mfma_f32_16x16x32_bf16 v[144:147], v[32:35], v[76:79], v[144:147]\n\t"
        "ds_read_b128 v[76:79], v29 offset:128\n\t"
        "s_waitcnt lgkmcnt(4)\n\t"
        "v_mfma_f32_16x16x32_bf16 v[164:167], v[40:43], v[80:83], v[164:167]\n\t"
        "ds_read_b128 v[80:83], v30 offset:128\n\t"
        "v_xor_b32 v29, %[q16], v21\n\t"
        "v_mfma_f32_16x16x32_bf16 v[84:87], v[44:47], v[56:59], v[84:87]\n\t"
        "ds_read_b64_tr_b16 v[32:33], %[wa0] offset:12288\n\t"
        "v_xor_b32 v30, %[q16], v22\n\t"
        "v_mfma_f32_16x16x32_bf16 v[88:91], v[48:51], v[56:59], v[88:91]\n\t"
        "ds_read_b64_tr_b16 v[34:35], %[wa0] offset:13056\n\t"
        "v_mfma_f32_16x16x32_bf16 v[92:95], v[52:55], v[56:59], v[92:95]\n\t"
        "ds_read_b64_tr_b16 v[36:37], %[wa1] offset:12288\n\t"
        "v_mfma_f32_16x16x32_bf16 v[104:107], v[52:55], v[60:63], v[104:107]\n\t"
        "ds_read_b64_tr_b16 v[38:39], %[wa1] offset:13056\n\t"
        "v_mfma_f32_16x16x32_bf16 v[100:103], v[48:51], v[60:63], v[100:103]\n\t"
        "ds_read_b64_tr_b16 v[40:41], %[wa2] offset:12288\n\t"
        "s_waitcnt lgkmcnt(9)\n\t"
        "v_mfma_f32_16x16x32_bf16 v[96:99], v[44:47], v[60:63], v[96:99]\n\t"
        "ds_read_b64_tr_b16 v[42:43], %[wa2] offset:13056\n\t"
        "v_mfma_f32_16x16x32_bf16 v[108:111], v[44:47], v[64:67], v[108:111]\n\t"
        "v_mfma_f32_16x16x32_bf16 v[112:115], v[48:51], v[64:67], v[112:115]\n\t"
        "s_waitcnt lgkmcnt(9)\n\t"
        "v_mfma_f32_16x16x32_bf16 v[116:119], v[52:55], v[64:67], v[116:119]\n\t"
        "s_waitcnt vmcnt(0) lgkmcnt(0)\n\t"
        "s_barrier\n\t"
        "s_cbranch_vccnz 411f\n\t"
        "412:\n\t"
        "v_mfma_f32_16x16x32_bf16 v[128:131], v[52:55], v[68:71], v[128:131]\n\t"
        "ds_read_b128 v[56:59], v24 offset:0\n\t"
        "v_mfma_f32_16x16x32_bf16 v[124:127], v[48:51], v[68:71], v[124:127]\n\t"
        "ds_read_b128 v[60:63], v25 offset:0\n\t"
        "v_mfma_f32_16x16x32_bf16 v[120:123], v[44:47], v[68:71], v[120:123]\n\t"
        "ds_read_b128 v[64:67], v26 offset:0\n\t"
        "v_mfma_f32_16x16x32_bf16 v[132:135], v[44:47], v[72:75], v[132:135]\n\t"
        "ds_read_b128 v[68:71], v27 offset:0\n\t"
        "v_mfma_f32_16x16x32_bf16 v[136:139], v[48:51], v[72:75], v[136:139]\n\t"
        "v_mfma_f32_16x16x32_bf16 v[140:143], v[52:55], v[72:75], v[140:143]\n\t"
        "ds_read_b128 v[72:75], v28 offset:0\n\t"
        "v_mfma_f32_16x16x32_bf16 v[152:155], v[52:55], v[76:79], v[152:155]\n\t"
        "v_mfma_f32_16x16x32_bf16 v[148:151], v[48:51], v[76:79], v[148:151]\n\t"
        "v_mfma_f32_16x16x32_bf16 v[144:147], v[44:47], v[76:79], v[144:147]\n\t"
        "ds_read_b128 v[76:79], v29 offset:0\n\t"
        "v_mfma_f32_16x16x32_bf16 v[164:167], v[52:55], v[80:83], v[164:167]\n\t"
        "ds_read_b128 v[80:83], v30 offset:0\n\t"
        "s_waitcnt lgkmcnt(6)\n\t"
        "s_branch 40b\n\t"
        "401:\n\t"
        "s_barrier\n\t"
        "s_branch 402b\n\t"
        "411:\n\t"
        "s_barrier\n\t"
        "s_branch 412b\n\t"
        "9:\n\t"
        "s_waitcnt lgkmcnt(0)\n\t"
        "s_nop 7\n\t"
        "v_mbcnt_lo_u32_b32 v12, -1, 0\n\t"
        "v_mbcnt_hi_u32_b32 v12, -1, v12\n\t"
        "v_lshl_add_u32 v12, v12, 3, %[xb]\n\t"
        "s_cmp_eq_u32 %[nh], 0\n\t"
        "s_cbranch_scc0 91f\n\t"
        "v_cvt_pk_bf16_f32 v32, v104, v105\n\t"
        "v_cvt_pk_bf16_f32 v33, v106, v107\n\t"
        "ds_write_b64 v12, v[32:33] offset:512\n\t"
        "v_cvt_pk_bf16_f32 v34, v128, v129\n\t"
        "v_cvt_pk_bf16_f32 v35, v130, v131\n\t"
        "ds_write_b64 v12, v[34:35] offset:1536\n\t"
        "v_cvt_pk_bf16_f32 v36, v152, v153\n\t"
        "v_cvt_pk_bf16_f32 v37, v154, v155\n\t"
        "ds_write_b64 v12, v[36:37] offset:2560\n\t"
        "v_cvt_pk_bf16_f32 v16, v92, v93\n\t"
        "v_cvt_pk_bf16_f32 v17, v94, v95\n\t"
        "v_cvt_pk_bf16_f32 v20, v116, v117\n\t"
        "v_cvt_pk_bf16_f32 v21, v118, v119\n\t"
        "v_cvt_pk_bf16_f32 v24, v140, v141\n\t"
        "v_cvt_pk_bf16_f32 v25, v142, v143\n\t"
        "v_cvt_pk_bf16_f32 v28, v164, v165\n\t"
        "v_cvt_pk_bf16_f32 v29, v166, v167\n\t"
        "s_branch 92f\n\t"
        "91:\n\t"
        "v_cvt_pk_bf16_f32 v32, v92, v93\n\t"
        "v_cvt_pk_bf16_f32 v33, v94, v95\n\t"
        "ds_write_b64 v12, v[32:33] offset:0\n\t"
        "v_cvt_pk_bf16_f32 v34, v116, v117\n\t"
        "v_cvt_pk_bf16_f32 v35, v118, v119\n\t"
        "ds_write_b64 v12, v[34:35] offset:1024\n\t"
        "v_cvt_pk_bf16_f32 v36, v140, v141\n\t"
        "v_cvt_pk_bf16_f32 v37, v142, v143\n\t"
        "ds_write_b64 v12, v[36:37] offset:2048\n\t"
        "v_cvt_pk_bf16_f32 v38, v164, v165\n\t"
        "v_cvt_pk_bf16_f32 v39, v166, v167\n\t"
        "ds_write_b64 v12, v[38:39] offset:3072\n\t"
        "v_cvt_pk_bf16_f32 v18, v104, v105\n\t"
        "v_cvt_pk_bf16_f32 v19, v106, v107\n\t"
        "v_cvt_pk_bf16_f32 v22, v128, v129\n\t"
        "v_cvt_pk_bf16_f32 v23, v130, v131\n\t"
        "v_cvt_pk_bf16_f32 v26, v152, v153\n\t"
        "v_cvt_pk_bf16_f32 v27, v154, v155\n\t"
        "92:\n\t"
        "s_mov_b32 %[tn], 0\n\t"
        "v_cvt_pk_bf16_f32 v40, v84, v85\n\t"
        "v_cvt_pk_bf16_f32 v41, v86, v87\n\t"
        "v_cvt_pk_bf16_f32 v42, v88, v89\n\t"
        "v_cvt_pk_bf16_f32 v43, v90, v91\n\t"
        "s_nop 1\n\t"
        "v_permlane16_swap_b32 v40, v42\n\t"
        "v_permlane16_swap_b32 v41, v43\n\t"
        "buffer_store_dwordx4 v[40:43], %[vo], %[ro], %[tn] offen sc1\n\t"
        "s_add_u32 %[tn], %[tn], 43008\n\t"
        "v_cvt_pk_bf16_f32 v44, v96, v97\n\t"
        "v_cvt_pk_bf16_f32 v45, v98, v99\n\t"
        "v_cvt_pk_bf16_f32 v46, v100, v101\n\t"
        "v_cvt_pk_bf16_f32 v47, v102, v103\n\t"
        "s_nop 1\n\t"
        "v_permlane16_swap_b32 v44, v46\n\t"
        "v_permlane16_swap_b32 v45, v47\n\t"
        "buffer_store_dwordx4 v[44:47], %[vo], %[ro], %[tn] offen sc1\n\t"
        "s_add_u32 %[tn], %[tn], 43008\n\t"
        "v_cvt_pk_bf16_f32 v48, v108, v109\n\t"
        "v_cvt_pk_bf16_f32 v49, v110, v111\n\t"
        "v_cvt_pk_bf16_f32 v50, v112, v113\n\t"
        "v_cvt_pk_bf16_f32 v51, v114, v115\n\t"
        "s_nop 1\n\t"
        "v_permlane16_swap_b32 v48, v50\n\t"
        "v_permlane16_swap_b32 v49, v51\n\t"
        "buffer_store_dwordx4 v[48:51], %[vo], %[ro], %[tn] offen sc1\n\t"
        "s_add_u32 %[tn], %[tn], 43008\n\t"
        "v_cvt_pk_bf16_f32 v52, v120, v121\n\t"
        "v_cvt_pk_bf16_f32 v53, v122, v123\n\t"
        "v_cvt_pk_bf16_f32 v54, v124, v125\n\t"
        "v_cvt_pk_bf16_f32 v55, v126, v127\n\t"
        "s_nop 1\n\t"
        "v_permlane16_swap_b32 v52, v54\n\t"
        "v_permlane16_swap_b32 v53, v55\n\t"
        "buffer_store_dwordx4 v[52:55], %[vo], %[ro], %[tn] offen sc1\n\t"
        "s_add_u32 %[tn], %[tn], 43008\n\t"
        "v_cvt_pk_bf16_f32 v56, v132, v133\n\t"
        "v_cvt_pk_bf16_f32 v57, v134, v135\n\t"
        "v_cvt_pk_bf16_f32 v58, v136, v137\n\t"
        "v_cvt_pk_bf16_f32 v59, v138, v139\n\t"
        "s_nop 1\n\t"
        "v_permlane16_swap_b32 v56, v58\n\t"
        "v_permlane16_swap_b32 v57, v59\n\t"
        "buffer_store_dwordx4 v[56:59], %[vo], %[ro], %[tn] offen sc1\n\t"
        "s_add_u32 %[tn], %[tn], 43008\n\t"
        "v_cvt_pk_bf16_f32 v60, v144, v145\n\t"
        "v_cvt_pk_bf16_f32 v61, v146, v147\n\t"
        "v_cvt_pk_bf16_f32 v62, v148, v149\n\t"
        "v_cvt_pk_bf16_f32 v63, v150, v151\n\t"
        "s_nop 1\n\t"
        "v_permlane16_swap_b32 v60, v62\n\t"
        "v_permlane16_swap_b32 v61, v63\n\t"
        "buffer_store_dwordx4 v[60:63], %[vo], %[ro], %[tn] offen sc1\n\t"
        "s_add_u32 %[tn], %[tn], 43008\n\t"
        "s_bitcmp0_b32 %[cv], 0\n\t"
        "s_cbranch_scc1 93f\n\t"
        "v_cvt_pk_bf16_f32 v64, v156, v157\n\t"
        "v_cvt_pk_bf16_f32 v65, v158, v159\n\t"
        "v_cvt_pk_bf16_f32 v66, v160, v161\n\t"
        "v_cvt_pk_bf16_f32 v67, v162, v163\n\t"
        "s_nop 1\n\t"
        "v_permlane16_swap_b32 v64, v66\n\t"
        "v_permlane16_swap_b32 v65, v67\n\t"
        "buffer_store_dwordx4 v[64:67], %[vo], %[ro], %[tn] offen sc1\n\t"
        "s_add_u32 %[tn], %[tn], 43008\n\t"
        "93:\n\t"
        "s_waitcnt lgkmcnt(0)\n\t"
        "s_barrier\n\t"
        "s_cmp_eq_u32 %[nh], 0\n\t"
        "s_cbranch_scc0 94f\n\t"
        "ds_read_b64 v[18:19], v12 offset:0\n\t"
        "ds_read_b64 v[22:23], v12 offset:1024\n\t"
        "ds_read_b64 v[26:27], v12 offset:2048\n\t"
        "ds_read_b64 v[30:31], v12 offset:3072\n\t"
        "s_branch 95f\n\t"
        "94:\n\t"
        "ds_read_b64 v[16:17], v12 offset:512\n\t"
        "ds_read_b64 v[20:21], v12 offset:1536\n\t"
        "ds_read_b64 v[24:25], v12 offset:2560\n\t"
        "95:\n\t"
        "s_cmp_eq_u32 %[cv], 4\n\t"
        "s_cselect_b32 s40, -301056, 0\n\t"
        "v_add_u32 v13, s40, %[vo2]\n\t"
        "s_lshl3_add_u32 s40, %[nh], %[cv]\n\t"
        "s_mov_b32 %[tn], 0\n\t"
        "s_waitcnt lgkmcnt(0)\n\t"
        "v_permlane16_swap_b32 v16, v18\n\t"
        "v_permlane16_swap_b32 v17, v19\n\t"
        "buffer_store_dwordx4 v[16:19], %[vo2], %[ro], %[tn] offen sc1\n\t"
        "s_add_u32 %[tn], %[tn], 86016\n\t"
        "v_permlane16_swap_b32 v20, v22\n\t"
        "v_permlane16_swap_b32 v21, v23\n\t"
        "buffer_store_dwordx4 v[20:23], %[vo2], %[ro], %[tn] offen sc1\n\t"
        "s_add_u32 %[tn], %[tn], 86016\n\t"
        "v_permlane16_swap_b32 v24, v26\n\t"
        "v_permlane16_swap_b32 v25, v27\n\t"
        "buffer_store_dwordx4 v[24:27], %[vo2], %[ro], %[tn] offen sc1\n\t"
        "s_add_u32 %[tn], %[tn], 86016\n\t"
        "s_bitcmp0_b32 0x90, s40\n\t"
        "s_cbranch_scc1 8f\n\t"
        "v_permlane16_swap_b32 v28, v30\n\t"
        "v_permlane16_swap_b32 v29, v31\n\t"
        "buffer_store_dwordx4 v[28:31], v13, %[ro], %[tn] offen sc1\n\t"
        "s_add_u32 %[tn], %[tn], 86016\n\t"
        "8:\n\t"
                : [ca] "+&v"(ca), [tn] "+&s"(tn)
                : [wa0] "v"(wa0), [wa1] "v"(wa0 ^ 32), [wa2] "v"((wb + 64) ^ (32 * nh)), [ta] "v"(TBL + (1 - mg) * 512 + p * 16), [q16] "v"(q << 4),
                  [vo] "v"(ob + 128 * nh + ((q & 1) << 5)), [vo2] "v"(ob + 64 + ((q & 1) << 5) + nh * ROWB),
                  [ro] "s"(ro), [cv] "s"(R == 13 ? 5 - mg : R >> 1), [nh] "s"(nh), [xb] "s"(POOL + mg * 3584), [xt] "s"(POOL + 24576 + mg * 8192), [xl] "s"(LIST + mg * 4096),
                  [xr] "s"(xr), [px] "v"(p + wx0), [rb] "v"(mg * 7 * 1280 + p * 64), RESTART_IN
                : "memory", "vcc", "scc", "s60", "s61", "s62", "v12", "v13", "v14", "v15", "v16", "v17", "v18", "v19", "v20", "v21", "v22", "v23", "v24", "v25", "v26", "v27", "v28", "v29", "v30", "v31", "v32", "v33", "v34", "v35", "v36", "v37", "v38", "v39", "v40", "v41", "v42", "v43", "v44", "v45", "v46", "v47", "v48", "v49", "v50", "v51", "v52", "v53", "v54", "v55", "v56", "v57", "v58", "v59", "v60", "v61", "v62", "v63", "v64", "v65", "v66", "v67", "v68", "v69", "v70", "v71", "v72", "v73", "v74", "v75", "v76", "v77", "v78", "v79", "v80", "v81", "v82", "v83", "v84", "v85", "v86", "v87", "v88", "v89", "v90", "v91", "v92", "v93", "v94", "v95", "v96", "v97", "v98", "v99", "v100", "v101", "v102", "v103", "v104", "v105", "v106", "v107", "v108", "v109", "v110", "v111", "v112", "v113", "v114", "v115", "v116", "v117", "v118", "v119", "v120", "v121", "v122", "v123", "v124", "v125", "v126", "v127", "v128", "v129", "v130", "v131", "v132", "v133", "v134", "v135", "v136", "v137", "v138", "v139", "v140", "v141", "v142", "v143", "v144", "v145", "v146", "v147", "v148", "v149", "v150", "v151", "v152", "v153", "v154", "v155", "v156", "v157", "v158", "v159", "v160", "v161", "v162", "v163", "v164", "v165", "v166", "v167", "s40", "s41", "s42");
        } else {
            LANE_VARS;
            u32 ca = CNT;
            const u32 wb = WB;
            const BatchCtx bc = batch_ctx(lane, pitch);
            asm volatile(
        "ds_read_b128 v[16:19], %[ta]\n\t"
        "ds_read_b32 v13, %[ca]\n\t"
        "ds_read_b64_tr_b16 v[32:33], %[wa0] offset:24576\n\t"
        "ds_read_b64_tr_b16 v[34:35], %[wa0] offset:25344\n\t"
        "ds_read_b64_tr_b16 v[36:37], %[wa1] offset:24576\n\t"
        "ds_read_b64_tr_b16 v[38:39], %[wa1] offset:25344\n\t"
        "ds_read_b64_tr_b16 v[40:41], %[wa0] offset:24640\n\t"
        "ds_read_b64_tr_b16 v[42:43], %[wa0] offset:25408\n\t"
        "ds_read_b64_tr_b16 v[44:45], %[wa1] offset:24640\n\t"
        "ds_read_b64_tr_b16 v[46:47], %[wa1] offset:25408\n\t"
        "ds_read_b64_tr_b16 v[48:49], %[wa0] offset:24704\n\t"
        "ds_read_b64_tr_b16 v[50:51], %[wa0] offset:25472\n\t"
        "ds_read_b64_tr_b16 v[52:53], %[wa1] offset:24704\n\t"
        "ds_read_b64_tr_b16 v[54:55], %[wa1] offset:25472\n\t"
        "v_mov_b64 v[104:105], 0\n\t"
        "v_mov_b64 v[106:107], 0\n\t"
        "v_mov_b64 v[108:109], 0\n\t"
        "v_mov_b64 v[110:111], 0\n\t"
        "v_mov_b64 v[112:113], 0\n\t"
        "v_mov_b64 v[114:115], 0\n\t"
        "v_mov_b64 v[116:117], 0\n\t"
        "v_mov_b64 v[118:119], 0\n\t"
        "v_mov_b64 v[120:121], 0\n\t"
        "v_mov_b64 v[122:123], 0\n\t"
        "v_mov_b64 v[124:125], 0\n\t"
        "v_mov_b64 v[126:127], 0\n\t"
        "v_mov_b64 v[128:129], 0\n\t"
        "v_mov_b64 v[130:131], 0\n\t"
        "v_mov_b64 v[132:133], 0\n\t"
        "v_mov_b64 v[134:135], 0\n\t"
        "v_mov_b64 v[136:137], 0\n\t"
        "v_mov_b64 v[138:139], 0\n\t"
        "v_mov_b64 v[140:141], 0\n\t"
        "v_mov_b64 v[142:143], 0\n\t"
        "v_mov_b64 v[144:145], 0\n\t"
        "v_mov_b64 v[146:147], 0\n\t"
        "v_mov_b64 v[148:149], 0\n\t"
        "v_mov_b64 v[150:151], 0\n\t"
        "s_waitcnt lgkmcnt(12)\n\t"
        "v_cmp_lt_u16 vcc, 0, v13\n\t"
        "v_xor_b32 v16, %[cx], v16\n\t"
        "v_mad_u32_u16 v20, v16, 16, 0\n\t"
        "v_mad_u32_u16 v21, v16, 16, 0 op_sel:[1,0,0,0]\n\t"
        "v_xor_b32 v18, %[cx], v18\n\t"
        "v_mad_u32_u16 v22, v18, 16, 0\n\t"
        "v_mad_u32_u16 v23, v18, 16, 0 op_sel:[1,0,0,0]\n\t"
        "v_perm_b32 v24, v17, v17, %[sel0]\n\t"
        "v_perm_b32 v25, v17, v17, %[sel1]\n\t"
        "v_perm_b32 v26, v17, v17, %[sel2]\n\t"
        "v_perm_b32 v27, v17, v17, %[sel3]\n\t"
        "v_perm_b32 v28, v19, v19, %[sel0]\n\t"
        "v_perm_b32 v29, v19, v19, %[sel1]\n\t"
        "v_perm_b32 v30, v19, v19, %[sel2]\n\t"
        "v_perm_b32 v31, v19, v19, %[sel3]\n\t"
        "s_waitcnt vmcnt(3) lgkmcnt(0)\n\t"
        "s_barrier\n\t"
        "s_cbranch_vccz 3f\n\t"
        "s_barrier\n\t"
        "3:\n\t"
        "s_cmp_eq_u32 %[nv], 1\n\t"
        "s_cbranch_scc1 15f\n\t"
        "25:\n\t"
        "s_setprio 2\n\t"
        "ds_read_b64_tr_b16 v[72:73], v20 offset:0\n\t"
        "ds_read_b64_tr_b16 v[74:75], v21 offset:0\n\t"
        "ds_read_b64_tr_b16 v[76:77], v20 offset:8\n\t"
        "ds_read_b64_tr_b16 v[78:79], v21 offset:8\n\t"
        "ds_read_b64_tr_b16 v[80:81], v22 offset:0\n\t"
        "ds_read_b64_tr_b16 v[82:83], v23 offset:0\n\t"
        "ds_read_b64_tr_b16 v[84:85], v22 offset:8\n\t"
        "ds_read_b64_tr_b16 v[86:87], v23 offset:8\n\t"
        "s_waitcnt lgkmcnt(0)\n\t"
        "v_mfma_f32_16x16x32_bf16 v[88:91], v[72:75], v[24:27], 0\n\t"
        "v_mfma_f32_16x16x32_bf16 v[92:95], v[76:79], v[24:27], 0\n\t"
        "v_mfma_f32_16x16x32_bf16 v[96:99], v[80:83], v[28:31], 0\n\t"
        "v_mfma_f32_16x16x32_bf16 v[100:103], v[84:87], v[28:31], 0\n\t"
        "ds_read_b64_tr_b16 v[72:73], v20 offset:64\n\t"
        "ds_read_b64_tr_b16 v[74:75], v21 offset:64\n\t"
        "ds_read_b64_tr_b16 v[76:77], v20 offset:72\n\t"
        "ds_read_b64_tr_b16 v[78:79], v21 offset:72\n\t"
        "ds_read_b64_tr_b16 v[80:81], v22 offset:64\n\t"
        "ds_read_b64_tr_b16 v[82:83], v23 offset:64\n\t"
        "ds_read_b64_tr_b16 v[84:85], v22 offset:72\n\t"
        "ds_read_b64_tr_b16 v[86:87], v23 offset:72\n\t"
        "ds_read2_b32 v[14:15], %[ca] offset0:1 offset1:2\n\t"
        "ds_read_b128 v[16:19], %[ta] offset:1024\n\t"
        "s_nop 5\n\t"
        "v_cvt_pk_bf16_f32 v56, v88, v89\n\t"
        "v_cvt_pk_bf16_f32 v57, v90, v91\n\t"
        "v_cvt_pk_bf16_f32 v58, v92, v93\n\t"
        "v_cvt_pk_bf16_f32 v59, v94, v95\n\t"
        "v_cvt_pk_bf16_f32 v60, v96, v97\n\t"
        "v_cvt_pk_bf16_f32 v61, v98, v99\n\t"
        "v_cvt_pk_bf16_f32 v62, v100, v101\n\t"
        "v_cvt_pk_bf16_f32 v63, v102, v103\n\t"
        "s_waitcnt lgkmcnt(0)\n\t"
        "s_cmp_lg_u32 %[rr], 8\n\t"
        "s_cbranch_scc1 20f\n\t"
        ".p2align 6\n\t"
        "30:\n\t"
        "s_waitcnt lgkmcnt(14)\n\t"
        "v_mfma_f32_16x16x32_bf16 v[88:91], v[72:75], v[24:27], 0\n\t"
        "ds_read_b64_tr_b16 v[54:55], %[wa1] offset:25472\n\t"
        "ds_read2_b32 v[14:15], %[ca] offset0:1 offset1:2\n\t"
        "v_mfma_f32_16x16x32_bf16 v[92:95], v[76:79], v[24:27], 0\n\t"
        "ds_read_b128 v[16:19], %[ta] offset:1024\n\t"
        "s_waitcnt lgkmcnt(14)\n\t"
        "v_mfma_f32_16x16x32_bf16 v[96:99], v[80:83], v[28:31], 0\n\t"
        "ds_read_b64_tr_b16 v[72:73], v20 offset:128\n\t"
        "ds_read_b64_tr_b16 v[74:75], v21 offset:128\n\t"
        "v_mfma_f32_16x16x32_bf16 v[100:103], v[84:87], v[28:31], 0\n\t"
        "ds_read_b64_tr_b16 v[76:77], v20 offset:136\n\t"
        "s_waitcnt lgkmcnt(13)\n\t"
        "v_mfma_f32_16x16x32_bf16 v[104:107], v[32:35], v[56:59], v[104:107]\n\t"
        "ds_read_b64_tr_b16 v[78:79], v21 offset:136\n\t"
        "v_mfma_f32_16x16x32_bf16 v[128:131], v[32:35], v[60:63], v[128:131]\n\t"
        "ds_read_b64_tr_b16 v[80:81], v22 offset:128\n\t"
        "ds_read_b64_tr_b16 v[82:83], v23 offset:128\n\t"
        "v_cvt_pk_bf16_f32 v64, v88, v89\n\t"
        "v_mfma_f32_16x16x32_bf16 v[132:135], v[36:39], v[60:63], v[132:135]\n\t"
        "ds_read_b64_tr_b16 v[84:85], v22 offset:136\n\t"
        "v_cvt_pk_bf16_f32 v65, v90, v91\n\t"
        "v_cvt_pk_bf16_f32 v66, v92, v93\n\t"
        "v_mfma_f32_16x16x32_bf16 v[108:111], v[36:39], v[56:59], v[108:111]\n\t"
        "ds_read_b64_tr_b16 v[86:87], v23 offset:136\n\t"
        "ds_read_b64_tr_b16 v[32:33], %[wa0] offset:30720\n\t"
        "v_cvt_pk_bf16_f32 v67, v94, v95\n\t"
        "s_waitcnt lgkmcnt(14)\n\t"
        "v_mfma_f32_16x16x32_bf16 v[112:115], v[40:43], v[56:59], v[112:115]\n\t"
        "ds_read_b64_tr_b16 v[34:35], %[wa0] offset:31488\n\t"
        "s_waitcnt lgkmcnt(10)\n\t"
        "v_cvt_pk_bf16_f32 v68, v96, v97\n\t"
        "v_mfma_f32_16x16x32_bf16 v[136:139], v[40:43], v[60:63], v[136:139]\n\t"
        "ds_read_b64_tr_b16 v[36:37], %[wa1] offset:30720\n\t"
        "v_add_u32 v13, v13, v14\n\t"
        "v_cvt_pk_bf16_f32 v69, v98, v99\n\t"
        "v_mfma_f32_16x16x32_bf16 v[140:143], v[44:47], v[60:63], v[140:143]\n\t"
        "ds_read_b64_tr_b16 v[38:39], %[wa1] offset:31488\n\t"
        "ds_read_b64_tr_b16 v[40:41], %[wa0] offset:30784\n\t"
        "v_cvt_pk_bf16_f32 v70, v100, v101\n\t"
        "v_mfma_f32_16x16x32_bf16 v[116:119], v[44:47], v[56:59], v[116:119]\n\t"
        "ds_read_b64_tr_b16 v[42:43], %[wa0] offset:31552\n\t"
        "v_cmp_lt_u16 vcc, 208, v13\n\t"
        "v_cvt_pk_bf16_f32 v71, v102, v103\n\t"
        "v_mfma_f32_16x16x32_bf16 v[120:123], v[48:51], v[56:59], v[120:123]\n\t"
        "ds_read_b64_tr_b16 v[44:45], %[wa1] offset:30784\n\t"
        "ds_read_b64_tr_b16 v[46:47], %[wa1] offset:31552\n\t"
        "v_mfma_f32_16x16x32_bf16 v[144:147], v[48:51], v[60:63], v[144:147]\n\t"
        "ds_read_b64_tr_b16 v[48:49], %[wa0] offset:30848\n\t"
        "v_xor_b32 v16, %[cx], v16\n\t"
        "v_mfma_f32_16x16x32_bf16 v[148:151], v[52:55], v[60:63], v[148:151]\n\t"
        "ds_read_b64_tr_b16 v[50:51], %[wa0] offset:31616\n\t"
        "v_mad_u32_u16 v20, v16, 16, 0\n\t"
        "v_mfma_f32_16x16x32_bf16 v[124:127], v[52:55], v[56:59], v[124:127]\n\t"
        "ds_read_b64_tr_b16 v[52:53], %[wa1] offset:30848\n\t"
        "ds_read_b64_tr_b16 v[54:55], %[wa1] offset:31616\n\t"
        "s_waitcnt lgkmcnt(14)\n\t"
        "v_mfma_f32_16x16x32_bf16 v[88:91], v[72:75], v[24:27], 0\n\t"
        "v_mad_u32_u16 v21, v16, 16, 0 op_sel:[1,0,0,0]\n\t"
        "v_xor_b32 v18, %[cx], v18\n\t"
        "v_mfma_f32_16x16x32_bf16 v[92:95], v[76:79], v[24:27], 0\n\t"
        "v_mad_u32_u16 v22, v18, 16, 0\n\t"
        "v_mad_u32_u16 v23, v18, 16, 0 op_sel:[1,0,0,0]\n\t"
        "s_waitcnt lgkmcnt(12)\n\t"
        "v_mfma_f32_16x16x32_bf16 v[96:99], v[80:83], v[28:31], 0\n\t"
        "v_perm_b32 v24, v17, v17, %[sel0]\n\t"
        "v_perm_b32 v25, v17, v17, %[sel1]\n\t"
        "v_mfma_f32_16x16x32_bf16 v[100:103], v[84:87], v[28:31], 0\n\t"
        "s_waitcnt lgkmcnt(0)\n\t"
        "s_barrier\n\t"
        "s_cbranch_vccnz 301f\n\t"
        "302:\n\t"
        "v_mfma_f32_16x16x32_bf16 v[104:107], v[32:35], v[64:67], v[104:107]\n\t"
        "ds_read_b64_tr_b16 v[72:73], v20 offset:0\n\t"
        "v_perm_b32 v26, v17, v17, %[sel2]\n\t"
        "v_perm_b32 v28, v19, v19, %[sel0]\n\t"
        "v_mfma_f32_16x16x32_bf16 v[128:131], v[32:35], v[68:71], v[128:131]\n\t"
        "ds_read_b64_tr_b16 v[74:75], v21 offset:0\n\t"
        "v_cvt_pk_bf16_f32 v56, v88, v89\n\t"
        "v_cvt_pk_bf16_f32 v57, v90, v91\n\t"
        "v_mfma_f32_16x16x32_bf16 v[132:135], v[36:39], v[68:71], v[132:135]\n\t"
        "ds_read_b64_tr_b16 v[76:77], v20 offset:8\n\t"
        "ds_read_b64_tr_b16 v[78:79], v21 offset:8\n\t"
        "v_cvt_pk_bf16_f32 v58, v92, v93\n\t"
        "v_mfma_f32_16x16x32_bf16 v[108:111], v[36:39], v[64:67], v[108:111]\n\t"
        "ds_read_b64_tr_b16 v[80:81], v22 offset:0\n\t"
        "v_cvt_pk_bf16_f32 v59, v94, v95\n\t"
        "v_cvt_pk_bf16_f32 v60, v96, v97\n\t"
        "v_mfma_f32_16x16x32_bf16 v[112:115], v[40:43], v[64:67], v[112:115]\n\t"
        "ds_read_b64_tr_b16 v[82:83], v23 offset:0\n\t"
        "v_cvt_pk_bf16_f32 v61, v98, v99\n\t"
        "v_cvt_pk_bf16_f32 v62, v100, v101\n\t"
        "v_mfma_f32_16x16x32_bf16 v[136:139], v[40:43], v[68:71], v[136:139]\n\t"
        "ds_read_b64_tr_b16 v[84:85], v22 offset:8\n\t"
        "ds_read_b64_tr_b16 v[86:87], v23 offset:8\n\t"
        "v_cvt_pk_bf16_f32 v63, v102, v103\n\t"
        "v_mfma_f32_16x16x32_bf16 v[140:143], v[44:47], v[68:71], v[140:143]\n\t"
        "ds_read_b64_tr_b16 v[32:33], %[wa0] offset:0\n\t"
        "v_perm_b32 v27, v17, v17, %[sel3]\n\t"
        "v_perm_b32 v29, v19, v19, %[sel1]\n\t"
        "v_mfma_f32_16x16x32_bf16 v[116:119], v[44:47], v[64:67], v[116:119]\n\t"
        "ds_read_b64_tr_b16 v[34:35], %[wa0] offset:768\n\t"
        "ds_read_b64_tr_b16 v[36:37], %[wa1] offset:0\n\t"
        "v_perm_b32 v30, v19, v19, %[sel2]\n\t"
        "v_mfma_f32_16x16x32_bf16 v[120:123], v[48:51], v[64:67], v[120:123]\n\t"
        "ds_read_b64_tr_b16 v[38:39], %[wa1] offset:768\n\t"
        "v_perm_b32 v31, v19, v19, %[sel3]\n\t"
        "v_mfma_f32_16x16x32_bf16 v[144:147], v[48:51], v[68:71], v[144:147]\n\t"
        "ds_read_b64_tr_b16 v[40:41], %[wa0] offset:64\n\t"
        "v_mfma_f32_16x16x32_bf16 v[148:151], v[52:55], v[68:71], v[148:151]\n\t"
        "ds_read_b64_tr_b16 v[42:43], %[wa0] offset:832\n\t"
        "ds_read_b64_tr_b16 v[44:45], %[wa1] offset:64\n\t"
        "v_mfma_f32_16x16x32_bf16 v[124:127], v[52:55], v[64:67], v[124:127]\n\t"
        "ds_read_b64_tr_b16 v[46:47], %[wa1] offset:832\n\t"
        "s_waitcnt lgkmcnt(12)\n\t"
        "v_mfma_f32_16x16x32_bf16 v[88:91], v[72:75], v[24:27], 0\n\t"
        "ds_read_b64_tr_b16 v[48:49], %[wa0] offset:128\n\t"
        "ds_read_b64_tr_b16 v[50:51], %[wa0] offset:896\n\t"
        "v_mfma_f32_16x16x32_bf16 v[92:95], v[76:79], v[24:27], 0\n\t"
        "ds_read_b64_tr_b16 v[52:53], %[wa1] offset:128\n\t"
        "s_waitcnt lgkmcnt(11)\n\t"
        "v_mfma_f32_16x16x32_bf16 v[96:99], v[80:83], v[28:31], 0\n\t"
        "ds_read_b64_tr_b16 v[54:55], %[wa1] offset:896\n\t"
        "v_mfma_f32_16x16x32_bf16 v[100:103], v[84:87], v[28:31], 0\n\t"
        "ds_read_b64_tr_b16 v[72:73], v20 offset:64\n\t"
        "ds_read_b64_tr_b16 v[74:75], v21 offset:64\n\t"
        "s_waitcnt lgkmcnt(10)\n\t"
        "v_mfma_f32_16x16x32_bf16 v[104:107], v[32:35], v[56:59], v[104:107]\n\t"
        "ds_read_b64_tr_b16 v[76:77], v20 offset:72\n\t"
        "v_mfma_f32_16x16x32_bf16 v[128:131], v[32:35], v[60:63], v[128:131]\n\t"
        "ds_read_b64_tr_b16 v[78:79], v21 offset:72\n\t"
        "ds_read_b64_tr_b16 v[80:81], v22 offset:64\n\t"
        "v_cvt_pk_bf16_f32 v64, v88, v89\n\t"
        "v_mfma_f32_16x16x32_bf16 v[132:135], v[36:39], v[60:63], v[132:135]\n\t"
        "ds_read_b64_tr_b16 v[82:83], v23 offset:64\n\t"
        "v_cvt_pk_bf16_f32 v65, v90, v91\n\t"
        "v_cvt_pk_bf16_f32 v66, v92, v93\n\t"
        "v_mfma_f32_16x16x32_bf16 v[108:111], v[36:39], v[56:59], v[108:111]\n\t"
        "ds_read_b64_tr_b16 v[84:85], v22 offset:72\n\t"
        "v_cvt_pk_bf16_f32 v67, v94, v95\n\t"
        "v_cvt_pk_bf16_f32 v68, v96, v97\n\t"
        "s_waitcnt lgkmcnt(11)\n\t"
        "v_mfma_f32_16x16x32_bf16 v[112:115], v[40:43], v[56:59], v[112:115]\n\t"
        "ds_read_b64_tr_b16 v[86:87], v23 offset:72\n\t"
        "ds_read_b64_tr_b16 v[32:33], %[wa0] offset:6144\n\t"
        "v_cvt_pk_bf16_f32 v69, v98, v99\n\t"
        "v_mfma_f32_16x16x32_bf16 v[136:139], v[40:43], v[60:63], v[136:139]\n\t"
        "ds_read_b64_tr_b16 v[34:35], %[wa0] offset:6912\n\t"
        "v_cvt_pk_bf16_f32 v70, v100, v101\n\t"
        "v_cvt_pk_bf16_f32 v71, v102, v103\n\t"
        "v_mfma_f32_16x16x32_bf16 v[140:143], v[44:47], v[60:63], v[140:143]\n\t"
        "ds_read_b64_tr_b16 v[36:37], %[wa1] offset:6144\n\t"
        "ds_read_b64_tr_b16 v[38:39], %[wa1] offset:6912\n\t"
        "v_mfma_f32_16x16x32_bf16 v[116:119], v[44:47], v[56:59], v[116:119]\n\t"
        "ds_read_b64_tr_b16 v[40:41], %[wa0] offset:6208\n\t"
        "s_waitcnt lgkmcnt(13)\n\t"
        "v_mfma_f32_16x16x32_bf16 v[120:123], v[48:51], v[56:59], v[120:123]\n\t"
        "ds_read_b64_tr_b16 v[42:43], %[wa0] offset:6976\n\t"
        "v_mfma_f32_16x16x32_bf16 v[144:147], v[48:51], v[60:63], v[144:147]\n\t"
        "ds_read_b64_tr_b16 v[44:45], %[wa1] offset:6208\n\t"
        "ds_read_b64_tr_b16 v[46:47], %[wa1] offset:6976\n\t"
        "v_mfma_f32_16x16x32_bf16 v[148:151], v[52:55], v[60:63], v[148:151]\n\t"
        "ds_read_b64_tr_b16 v[48:49], %[wa0] offset:6272\n\t"
        "s_sub_u32 %[tn], %[tn], 1\n\t"
        "v_mfma_f32_16x16x32_bf16 v[124:127], v[52:55], v[56:59], v[124:127]\n\t"
        "ds_read_b64_tr_b16 v[50:51], %[wa0] offset:7040\n\t"
        "ds_read_b64_tr_b16 v[52:53], %[wa1] offset:6272\n\t"
        "s_cbranch_scc1 9f\n\t"
        "s_waitcnt lgkmcnt(14)\n\t"
        "v_mfma_f32_16x16x32_bf16 v[88:91], v[72:75], v[24:27], 0\n\t"
        "ds_read_b64_tr_b16 v[54:55], %[wa1] offset:7040\n\t"
        "v_mfma_f32_16x16x32_bf16 v[92:95], v[76:79], v[24:27], 0\n\t"
        "ds_read_b128 v[16:19], %[ta] offset:0\n\t"
        "s_waitcnt lgkmcnt(13)\n\t"
        "v_mfma_f32_16x16x32_bf16 v[96:99], v[80:83], v[28:31], 0\n\t"
        "ds_read_b64_tr_b16 v[72:73], v20 offset:128\n\t"
        "ds_read_b64_tr_b16 v[74:75], v21 offset:128\n\t"
        "v_mfma_f32_16x16x32_bf16 v[100:103], v[84:87], v[28:31], 0\n\t"
        "ds_read_b64_tr_b16 v[76:77], v20 offset:136\n\t"
        "s_waitcnt lgkmcnt(12)\n\t"
        "v_mfma_f32_16x16x32_bf16 v[104:107], v[32:35], v[64:67], v[104:107]\n\t"
        "ds_read_b64_tr_b16 v[78:79], v21 offset:136\n\t"
        "ds_read_b64_tr_b16 v[80:81], v22 offset:128\n\t"
        "v_mfma_f32_16x16x32_bf16 v[128:131], v[32:35], v[68:71], v[128:131]\n\t"
        "ds_read_b64_tr_b16 v[82:83], v23 offset:128\n\t"
        "v_cvt_pk_bf16_f32 v56, v88, v89\n\t"
        "v_cvt_pk_bf16_f32 v57, v90, v91\n\t"
        "v_mfma_f32_16x16x32_bf16 v[132:135], v[36:39], v[68:71], v[132:135]\n\t"
        "ds_read_b64_tr_b16 v[84:85], v22 offset:136\n\t"
        "v_cvt_pk_bf16_f32 v58, v92, v93\n\t"
        "v_cvt_pk_bf16_f32 v59, v94, v95\n\t"
        "v_mfma_f32_16x16x32_bf16 v[108:111], v[36:39], v[64:67], v[108:111]\n\t"
        "ds_read_b64_tr_b16 v[86:87], v23 offset:136\n\t"
        "ds_read_b64_tr_b16 v[32:33], %[wa0] offset:12288\n\t"
        "v_cvt_pk_bf16_f32 v60, v96, v97\n\t"
        "s_waitcnt lgkmcnt(14)\n\t"
        "v_mfma_f32_16x16x32_bf16 v[112:115], v[40:43], v[64:67], v[112:115]\n\t"
        "ds_read_b64_tr_b16 v[34:35], %[wa0] offset:13056\n\t"
        "s_waitcnt lgkmcnt(10)\n\t"
        "v_cvt_pk_bf16_f32 v61, v98, v99\n\t"
        "v_mfma_f32_16x16x32_bf16 v[136:139], v[40:43], v[68:71], v[136:139]\n\t"
        "ds_read_b64_tr_b16 v[36:37], %[wa1] offset:12288\n\t"
        "ds_read_b64_tr_b16 v[38:39], %[wa1] offset:13056\n\t"
        "v_cvt_pk_bf16_f32 v62, v100, v101\n\t"
        "v_mfma_f32_16x16x32_bf16 v[140:143], v[44:47], v[68:71], v[140:143]\n\t"
        "ds_read_b64_tr_b16 v[40:41], %[wa0] offset:12352\n\t"
        "v_add_u32 v14, v14, v15\n\t"
        "v_cvt_pk_bf16_f32 v63, v102, v103\n\t"
        "v_mfma_f32_16x16x32_bf16 v[116:119], v[44:47], v[64:67], v[116:119]\n\t"
        "ds_read_b64_tr_b16 v[42:43], %[wa0] offset:13120\n\t"
        "v_cmp_lt_u16 vcc, 208, v14\n\t"
        "v_mfma_f32_16x16x32_bf16 v[120:123], v[48:51], v[64:67], v[120:123]\n\t"
        "ds_read_b64_tr_b16 v[44:45], %[wa1] offset:12352\n\t"
        "ds_read_b64_tr_b16 v[46:47], %[wa1] offset:13120\n\t"
        "v_mfma_f32_16x16x32_bf16 v[144:147], v[48:51], v[68:71], v[144:147]\n\t"
        "ds_read_b64_tr_b16 v[48:49], %[wa0] offset:12416\n\t"
        "v_mov_b32 v13, v15\n\t"
        "v_mfma_f32_16x16x32_bf16 v[148:151], v[52:55], v[68:71], v[148:151]\n\t"
        "ds_read_b64_tr_b16 v[50:51], %[wa0] offset:13184\n\t"
        "v_add_u32 %[ca], 8, %[ca]\n\t"
        "v_mfma_f32_16x16x32_bf16 v[124:127], v[52:55], v[64:67], v[124:127]\n\t"
        "ds_read_b64_tr_b16 v[52:53], %[wa1] offset:12416\n\t"
        "ds_read_b64_tr_b16 v[54:55], %[wa1] offset:13184\n\t"
        "s_waitcnt lgkmcnt(14)\n\t"
        "v_mfma_f32_16x16x32_bf16 v[88:91], v[72:75], v[24:27], 0\n\t"
        "v_xor_b32 v16, %[cx], v16\n\t"
        "v_mad_u32_u16 v20, v16, 16, 0\n\t"
        "v_mfma_f32_16x16x32_bf16 v[92:95], v[76:79], v[24:27], 0\n\t"
        "v_mad_u32_u16 v21, v16, 16, 0 op_sel:[1,0,0,0]\n\t"
        "v_xor_b32 v18, %[cx], v18\n\t"
        "s_waitcnt lgkmcnt(12)\n\t"
        "v_mfma_f32_16x16x32_bf16 v[96:99], v[80:83], v[28:31], 0\n\t"
        "v_mad_u32_u16 v22, v18, 16, 0\n\t"
        "v_mad_u32_u16 v23, v18, 16, 0 op_sel:[1,0,0,0]\n\t"
        "v_mfma_f32_16x16x32_bf16 v[100:103], v[84:87], v[28:31], 0\n\t"
        "s_waitcnt vmcnt(0) lgkmcnt(0)\n\t"
        "s_barrier\n\t"
        "s_cbranch_vccnz 311f\n\t"
        "312:\n\t"
        "v_mfma_f32_16x16x32_bf16 v[104:107], v[32:35], v[56:59], v[104:107]\n\t"
        "ds_read_b64_tr_b16 v[72:73], v20 offset:0\n\t"
        "v_perm_b32 v24, v17, v17, %[sel0]\n\t"
        "v_perm_b32 v28, v19, v19, %[sel0]\n\t"
        "v_mfma_f32_16x16x32_bf16 v[128:131], v[32:35], v[60:63], v[128:131]\n\t"
        "ds_read_b64_tr_b16 v[74:75], v21 offset:0\n\t"
        "v_cvt_pk_bf16_f32 v64, v88, v89\n\t"
        "v_cvt_pk_bf16_f32 v65, v90, v91\n\t"
        "v_mfma_f32_16x16x32_bf16 v[132:135], v[36:39], v[60:63], v[132:135]\n\t"
        "ds_read_b64_tr_b16 v[76:77], v20 offset:8\n\t"
        "ds_read_b64_tr_b16 v[78:79], v21 offset:8\n\t"
        "v_cvt_pk_bf16_f32 v66, v92, v93\n\t"
        "v_mfma_f32_16x16x32_bf16 v[108:111], v[36:39], v[56:59], v[108:111]\n\t"
        "ds_read_b64_tr_b16 v[80:81], v22 offset:0\n\t"
        "v_cvt_pk_bf16_f32 v67, v94, v95\n\t"
        "v_cvt_pk_bf16_f32 v68, v96, v97\n\t"
        "v_mfma_f32_16x16x32_bf16 v[112:115], v[40:43], v[56:59], v[112:115]\n\t"
        "ds_read_b64_tr_b16 v[82:83], v23 offset:0\n\t"
        "ds_read_b64_tr_b16 v[84:85], v22 offset:8\n\t"
        "v_cvt_pk_bf16_f32 v69, v98, v99\n\t"
        "v_mfma_f32_16x16x32_bf16 v[136:139], v[40:43], v[60:63], v[136:139]\n\t"
        "ds_read_b64_tr_b16 v[86:87], v23 offset:8\n\t"
        "v_cvt_pk_bf16_f32 v70, v100, v101\n\t"
        "v_cvt_pk_bf16_f32 v71, v102, v103\n\t"
        "v_mfma_f32_16x16x32_bf16 v[140:143], v[44:47], v[60:63], v[140:143]\n\t"
        "ds_read_b64_tr_b16 v[32:33], %[wa0] offset:18432\n\t"
        "v_perm_b32 v25, v17, v17, %[sel1]\n\t"
        "v_perm_b32 v29, v19, v19, %[sel1]\n\t"
        "v_mfma_f32_16x16x32_bf16 v[116:119], v[44:47], v[56:59], v[116:119]\n\t"
        "ds_read_b64_tr_b16 v[34:35], %[wa0] offset:19200\n\t"
        "ds_read_b64_tr_b16 v[36:37], %[wa1] offset:18432\n\t"
        "v_perm_b32 v30, v19, v19, %[sel2]\n\t"
        "v_mfma_f32_16x16x32_bf16 v[120:123], v[48:51], v[56:59], v[120:123]\n\t"
        "ds_read_b64_tr_b16 v[38:39], %[wa1] offset:19200\n\t"
        "v_perm_b32 v26, v17, v17, %[sel2]\n\t"
        "v_perm_b32 v31, v19, v19, %[sel3]\n\t"
        "v_mfma_f32_16x16x32_bf16 v[144:147], v[48:51], v[60:63], v[144:147]\n\t"
        "ds_read_b64_tr_b16 v[40:41], %[wa0] offset:18496\n\t"
        "ds_read_b64_tr_b16 v[42:43], %[wa0] offset:19264\n\t"
        "v_mfma_f32_16x16x32_bf16 v[148:151], v[52:55], v[60:63], v[148:151]\n\t"
        "ds_read_b64_tr_b16 v[44:45], %[wa1] offset:18496\n\t"
        "v_perm_b32 v27, v17, v17, %[sel3]\n\t"
        "v_mfma_f32_16x16x32_bf16 v[124:127], v[52:55], v[56:59], v[124:127]\n\t"
        "ds_read_b64_tr_b16 v[46:47], %[wa1] offset:19264\n\t"
        "ds_read_b64_tr_b16 v[48:49], %[wa0] offset:18560\n\t"
        "s_waitcnt lgkmcnt(13)\n\t"
        "v_mfma_f32_16x16x32_bf16 v[88:91], v[72:75], v[24:27], 0\n\t"
        "ds_read_b64_tr_b16 v[50:51], %[wa0] offset:19328\n\t"
        "v_mfma_f32_16x16x32_bf16 v[92:95], v[76:79], v[24:27], 0\n\t"
        "ds_read_b64_tr_b16 v[52:53], %[wa1] offset:18560\n\t"
        "s_waitcnt lgkmcnt(11)\n\t"
        "v_mfma_f32_16x16x32_bf16 v[96:99], v[80:83], v[28:31], 0\n\t"
        "ds_read_b64_tr_b16 v[54:55], %[wa1] offset:19328\n\t"
        "ds_read_b64_tr_b16 v[72:73], v20 offset:64\n\t"
        "v_mfma_f32_16x16x32_bf16 v[100:103], v[84:87], v[28:31], 0\n\t"
        "ds_read_b64_tr_b16 v[74:75], v21 offset:64\n\t"
        "s_waitcnt lgkmcnt(10)\n\t"
        "v_mfma_f32_16x16x32_bf16 v[104:107], v[32:35], v[64:67], v[104:107]\n\t"
        "ds_read_b64_tr_b16 v[76:77], v20 offset:72\n\t"
        "ds_read_b64_tr_b16 v[78:79], v21 offset:72\n\t"
        "v_mfma_f32_16x16x32_bf16 v[128:131], v[32:35], v[68:71], v[128:131]\n\t"
        "ds_read_b64_tr_b16 v[80:81], v22 offset:64\n\t"
        "v_cvt_pk_bf16_f32 v56, v88, v89\n\t"
        "v_cvt_pk_bf16_f32 v57, v90, v91\n\t"
        "v_mfma_f32_16x16x32_bf16 v[132:135], v[36:39], v[68:71], v[132:135]\n\t"
        "ds_read_b64_tr_b16 v[82:83], v23 offset:64\n\t"
        "v_cvt_pk_bf16_f32 v58, v92, v93\n\t"
        "v_cvt_pk_bf16_f32 v59, v94, v95\n\t"
        "v_mfma_f32_16x16x32_bf16 v[108:111], v[36:39], v[64:67], v[108:111]\n\t"
        "ds_read_b64_tr_b16 v[84:85], v22 offset:72\n\t"
        "ds_read_b64_tr_b16 v[86:87], v23 offset:72\n\t"
        "v_cvt_pk_bf16_f32 v60, v96, v97\n\t"
        "s_waitcnt lgkmcnt(12)\n\t"
        "v_mfma_f32_16x16x32_bf16 v[112:115], v[40:43], v[64:67], v[112:115]\n\t"
        "ds_read_b64_tr_b16 v[32:33], %[wa0] offset:24576\n\t"
        "v_cvt_pk_bf16_f32 v61, v98, v99\n\t"
        "v_cvt_pk_bf16_f32 v62, v100, v101\n\t"
        "v_mfma_f32_16x16x32_bf16 v[136:139], v[40:43], v[68:71], v[136:139]\n\t"
        "ds_read_b64_tr_b16 v[34:35], %[wa0] offset:25344\n\t"
        "ds_read_b64_tr_b16 v[36:37], %[wa1] offset:24576\n\t"
        "v_cvt_pk_bf16_f32 v63, v102, v103\n\t"
        "v_mfma_f32_16x16x32_bf16 v[140:143], v[44:47], v[68:71], v[140:143]\n\t"
        "ds_read_b64_tr_b16 v[38:39], %[wa1] offset:25344\n\t"
        "v_mfma_f32_16x16x32_bf16 v[116:119], v[44:47], v[64:67], v[116:119]\n\t"
        "ds_read_b64_tr_b16 v[40:41], %[wa0] offset:24640\n\t"
        "ds_read_b64_tr_b16 v[42:43], %[wa0] offset:25408\n\t"
        "s_waitcnt lgkmcnt(14)\n\t"
        "v_mfma_f32_16x16x32_bf16 v[120:123], v[48:51], v[64:67], v[120:123]\n\t"
        "ds_read_b64_tr_b16 v[44:45], %[wa1] offset:24640\n\t"
        "v_mfma_f32_16x16x32_bf16 v[144:147], v[48:51], v[68:71], v[144:147]\n\t"
        "ds_read_b64_tr_b16 v[46:47], %[wa1] offset:25408\n\t"
        "v_mfma_f32_16x16x32_bf16 v[148:151], v[52:55], v[68:71], v[148:151]\n\t"
        "ds_read_b64_tr_b16 v[48:49], %[wa0] offset:24704\n\t"
        "ds_read_b64_tr_b16 v[50:51], %[wa0] offset:25472\n\t"
        "v_mfma_f32_16x16x32_bf16 v[124:127], v[52:55], v[64:67], v[124:127]\n\t"
        "ds_read_b64_tr_b16 v[52:53], %[wa1] offset:24704\n\t"
        "s_branch 30b\n\t"
        "301:\n\t"
        "s_barrier\n\t"
        "s_branch 302b\n\t"
        "311:\n\t"
        "s_barrier\n\t"
        "s_branch 312b\n\t"
        ".p2align 6\n\t"
        "20:\n\t"
        "s_waitcnt lgkmcnt(14)\n\t"
        "v_mfma_f32_16x16x32_bf16 v[88:91], v[72:75], v[24:27], 0\n\t"
        "ds_read_b64_tr_b16 v[72:73], v20 offset:128\n\t"
        "ds_read_b64_tr_b16 v[74:75], v21 offset:128\n\t"
        "v_mfma_f32_16x16x32_bf16 v[92:95], v[76:79], v[24:27], 0\n\t"
        "ds_read_b64_tr_b16 v[76:77], v20 offset:136\n\t"
        "ds_read_b64_tr_b16 v[78:79], v21 offset:136\n\t"
        "s_waitcnt lgkmcnt(14)\n\t"
        "v_mfma_f32_16x16x32_bf16 v[96:99], v[80:83], v[28:31], 0\n\t"
        "ds_read_b64_tr_b16 v[80:81], v22 offset:128\n\t"
        "ds_read_b64_tr_b16 v[82:83], v23 offset:128\n\t"
        "v_mfma_f32_16x16x32_bf16 v[100:103], v[84:87], v[28:31], 0\n\t"
        "ds_read_b64_tr_b16 v[84:85], v22 offset:136\n\t"
        "ds_read_b64_tr_b16 v[86:87], v23 offset:136\n\t"
        "s_waitcnt lgkmcnt(14)\n\t"
        "v_mfma_f32_16x16x32_bf16 v[104:107], v[32:35], v[56:59], v[104:107]\n\t"
        "ds_read2_b32 v[14:15], %[ca] offset0:1 offset1:2\n\t"
        "ds_read_b128 v[16:19], %[ta] offset:1024\n\t"
        "v_mfma_f32_16x16x32_bf16 v[128:131], v[32:35], v[60:63], v[128:131]\n\t"
        "ds_read_b64_tr_b16 v[32:33], %[wa0] offset:30720\n\t"
        "v_cvt_pk_bf16_f32 v64, v88, v89\n\t"
        "v_cvt_pk_bf16_f32 v65, v90, v91\n\t"
        "v_mfma_f32_16x16x32_bf16 v[132:135], v[36:39], v[60:63], v[132:135]\n\t"
        "ds_read_b64_tr_b16 v[34:35], %[wa0] offset:31488\n\t"
        "v_cvt_pk_bf16_f32 v66, v92, v93\n\t"
        "v_cvt_pk_bf16_f32 v67, v94, v95\n\t"
        "v_mfma_f32_16x16x32_bf16 v[108:111], v[36:39], v[56:59], v[108:111]\n\t"
        "ds_read_b64_tr_b16 v[36:37], %[wa1] offset:30720\n\t"
        "v_cvt_pk_bf16_f32 v68, v96, v97\n\t"
        "v_cvt_pk_bf16_f32 v69, v98, v99\n\t"
        "s_waitcnt lgkmcnt(14)\n\t"
        "v_mfma_f32_16x16x32_bf16 v[112:115], v[40:43], v[56:59], v[112:115]\n\t"
        "ds_read_b64_tr_b16 v[38:39], %[wa1] offset:31488\n\t"
        "v_cvt_pk_bf16_f32 v70, v100, v101\n\t"
        "v_cvt_pk_bf16_f32 v71, v102, v103\n\t"
        "v_mfma_f32_16x16x32_bf16 v[136:139], v[40:43], v[60:63], v[136:139]\n\t"
        "ds_read_b64_tr_b16 v[40:41], %[wa0] offset:30784\n\t"
        "v_mfma_f32_16x16x32_bf16 v[140:143], v[44:47], v[60:63], v[140:143]\n\t"
        "ds_read_b64_tr_b16 v[42:43], %[wa0] offset:31552\n\t"
        "s_waitcnt lgkmcnt(6)\n\t"
        "v_mfma_f32_16x16x32_bf16 v[116:119], v[44:47], v[56:59], v[116:119]\n\t"
        "ds_read_b64_tr_b16 v[44:45], %[wa1] offset:30784\n\t"
        "v_add_u32 v13, v13, v14\n\t"
        "v_mfma_f32_16x16x32_bf16 v[120:123], v[48:51], v[56:59], v[120:123]\n\t"
        "ds_read_b64_tr_b16 v[46:47], %[wa1] offset:31552\n\t"
        "v_cmp_lt_u16 vcc, 208, v13\n\t"
        "v_mfma_f32_16x16x32_bf16 v[144:147], v[48:51], v[60:63], v[144:147]\n\t"
        "ds_read_b64_tr_b16 v[48:49], %[wa0] offset:30848\n\t"
        "v_xor_b32 v16, %[cx], v16\n\t"
        "v_mfma_f32_16x16x32_bf16 v[148:151], v[52:55], v[60:63], v[148:151]\n\t"
        "ds_read_b64_tr_b16 v[50:51], %[wa0] offset:31616\n\t"
        "v_mad_u32_u16 v20, v16, 16, 0\n\t"
        "v_mfma_f32_16x16x32_bf16 v[124:127], v[52:55], v[56:59], v[124:127]\n\t"
        "ds_read_b64_tr_b16 v[52:53], %[wa1] offset:30848\n\t"
        "ds_read_b64_tr_b16 v[54:55], %[wa1] offset:31616\n\t"
        "v_mfma_f32_16x16x32_bf16 v[88:91], v[72:75], v[24:27], 0\n\t"
        "v_mad_u32_u16 v21, v16, 16, 0 op_sel:[1,0,0,0]\n\t"
        "v_xor_b32 v18, %[cx], v18\n\t"
        "v_mfma_f32_16x16x32_bf16 v[92:95], v[76:79], v[24:27], 0\n\t"
        "v_mad_u32_u16 v22, v18, 16, 0\n\t"
        "v_mad_u32_u16 v23, v18, 16, 0 op_sel:[1,0,0,0]\n\t"
        "v_mfma_f32_16x16x32_bf16 v[96:99], v[80:83], v[28:31], 0\n\t"
        "v_perm_b32 v24, v17, v17, %[sel0]\n\t"
        "v_perm_b32 v25, v17, v17, %[sel1]\n\t"
        "v_mfma_f32_16x16x32_bf16 v[100:103], v[84:87], v[28:31], 0\n\t"
        "s_waitcnt lgkmcnt(0)\n\t"
        "s_barrier\n\t"
        "s_cbranch_vccnz 201f\n\t"
        "202:\n\t"
        "v_mfma_f32_16x16x32_bf16 v[104:107], v[32:35], v[64:67], v[104:107]\n\t"
        "ds_read_b64_tr_b16 v[72:73], v20 offset:0\n\t"
        "ds_read_b64_tr_b16 v[74:75], v21 offset:0\n\t"
        "v_perm_b32 v28, v19, v19, %[sel0]\n\t"
        "v_mfma_f32_16x16x32_bf16 v[128:131], v[32:35], v[68:71], v[128:131]\n\t"
        "ds_read_b64_tr_b16 v[76:77], v20 offset:8\n\t"
        "ds_read_b64_tr_b16 v[78:79], v21 offset:8\n\t"
        "v_cvt_pk_bf16_f32 v56, v88, v89\n\t"
        "v_mfma_f32_16x16x32_bf16 v[132:135], v[36:39], v[68:71], v[132:135]\n\t"
        "ds_read_b64_tr_b16 v[80:81], v22 offset:0\n\t"
        "ds_read_b64_tr_b16 v[82:83], v23 offset:0\n\t"
        "v_cvt_pk_bf16_f32 v57, v90, v91\n\t"
        "v_mfma_f32_16x16x32_bf16 v[108:111], v[36:39], v[64:67], v[108:111]\n\t"
        "ds_read_b64_tr_b16 v[84:85], v22 offset:8\n\t"
        "ds_read_b64_tr_b16 v[86:87], v23 offset:8\n\t"
        "v_cvt_pk_bf16_f32 v58, v92, v93\n\t"
        "v_mfma_f32_16x16x32_bf16 v[112:115], v[40:43], v[64:67], v[112:115]\n\t"
        "ds_read_b64_tr_b16 v[32:33], %[wa0] offset:0\n\t"
        "ds_read_b64_tr_b16 v[34:35], %[wa0] offset:768\n\t"
        "v_cvt_pk_bf16_f32 v59, v94, v95\n\t"
        "v_mfma_f32_16x16x32_bf16 v[136:139], v[40:43], v[68:71], v[136:139]\n\t"
        "ds_read_b64_tr_b16 v[36:37], %[wa1] offset:0\n\t"
        "ds_read_b64_tr_b16 v[38:39], %[wa1] offset:768\n\t"
        "v_cvt_pk_bf16_f32 v60, v96, v97\n\t"
        "v_mfma_f32_16x16x32_bf16 v[140:143], v[44:47], v[68:71], v[140:143]\n\t"
        "ds_read_b64_tr_b16 v[40:41], %[wa0] offset:64\n\t"
        "ds_read_b64_tr_b16 v[42:43], %[wa0] offset:832\n\t"
        "v_cvt_pk_bf16_f32 v61, v98, v99\n\t"
        "v_mfma_f32_16x16x32_bf16 v[116:119], v[44:47], v[64:67], v[116:119]\n\t"
        "ds_read_b64_tr_b16 v[44:45], %[wa1] offset:64\n\t"
        "ds_read_b64_tr_b16 v[46:47], %[wa1] offset:832\n\t"
        "v_cvt_pk_bf16_f32 v62, v100, v101\n\t"
        "v_mfma_f32_16x16x32_bf16 v[120:123], v[48:51], v[64:67], v[120:123]\n\t"
        "v_cvt_pk_bf16_f32 v63, v102, v103\n\t"
        "v_perm_b32 v26, v17, v17, %[sel2]\n\t"
        "v_perm_b32 v29, v19, v19, %[sel1]\n\t"
        "v_mfma_f32_16x16x32_bf16 v[144:147], v[48:51], v[68:71], v[144:147]\n\t"
        "ds_read_b64_tr_b16 v[48:49], %[wa0] offset:128\n\t"
        "ds_read_b64_tr_b16 v[50:51], %[wa0] offset:896\n\t"
        "v_perm_b32 v30, v19, v19, %[sel2]\n\t"
        "v_mfma_f32_16x16x32_bf16 v[148:151], v[52:55], v[68:71], v[148:151]\n\t"
        "v_perm_b32 v27, v17, v17, %[sel3]\n\t"
        "v_perm_b32 v31, v19, v19, %[sel3]\n\t"
        "v_mfma_f32_16x16x32_bf16 v[124:127], v[52:55], v[64:67], v[124:127]\n\t"
        "ds_read_b64_tr_b16 v[52:53], %[wa1] offset:128\n\t"
        "ds_read_b64_tr_b16 v[54:55], %[wa1] offset:896\n\t"
        "s_waitcnt lgkmcnt(14)\n\t"
        "v_mfma_f32_16x16x32_bf16 v[88:91], v[72:75], v[24:27], 0\n\t"
        "ds_read_b64_tr_b16 v[72:73], v20 offset:64\n\t"
        "ds_read_b64_tr_b16 v[74:75], v21 offset:64\n\t"
        "v_mfma_f32_16x16x32_bf16 v[92:95], v[76:79], v[24:27], 0\n\t"
        "ds_read_b64_tr_b16 v[76:77], v20 offset:72\n\t"
        "ds_read_b64_tr_b16 v[78:79], v21 offset:72\n\t"
        "s_waitcnt lgkmcnt(14)\n\t"
        "v_mfma_f32_16x16x32_bf16 v[96:99], v[80:83], v[28:31], 0\n\t"
        "ds_read_b64_tr_b16 v[80:81], v22 offset:64\n\t"
        "ds_read_b64_tr_b16 v[82:83], v23 offset:64\n\t"
        "v_mfma_f32_16x16x32_bf16 v[100:103], v[84:87], v[28:31], 0\n\t"
        "ds_read_b64_tr_b16 v[84:85], v22 offset:72\n\t"
        "ds_read_b64_tr_b16 v[86:87], v23 offset:72\n\t"
        "s_waitcnt lgkmcnt(14)\n\t"
        "v_mfma_f32_16x16x32_bf16 v[104:107], v[32:35], v[56:59], v[104:107]\n\t"
        "v_mfma_f32_16x16x32_bf16 v[128:131], v[32:35], v[60:63], v[128:131]\n\t"
        "ds_read_b64_tr_b16 v[32:33], %[wa0] offset:6144\n\t"
        "v_cvt_pk_bf16_f32 v64, v88, v89\n\t"
        "v_mfma_f32_16x16x32_bf16 v[132:135], v[36:39], v[60:63], v[132:135]\n\t"
        "ds_read_b64_tr_b16 v[34:35], %[wa0] offset:6912\n\t"
        "v_cvt_pk_bf16_f32 v65, v90, v91\n\t"
        "v_mfma_f32_16x16x32_bf16 v[108:111], v[36:39], v[56:59], v[108:111]\n\t"
        "ds_read_b64_tr_b16 v[36:37], %[wa1] offset:6144\n\t"
        "v_cvt_pk_bf16_f32 v66, v92, v93\n\t"
        "s_waitcnt lgkmcnt(14)\n\t"
        "v_mfma_f32_16x16x32_bf16 v[112:115], v[40:43], v[56:59], v[112:115]\n\t"
        "ds_read_b64_tr_b16 v[38:39], %[wa1] offset:6912\n\t"
        "v_cvt_pk_bf16_f32 v67, v94, v95\n\t"
        "v_mfma_f32_16x16x32_bf16 v[136:139], v[40:43], v[60:63], v[136:139]\n\t"
        "ds_read_b64_tr_b16 v[40:41], %[wa0] offset:6208\n\t"
        "v_cvt_pk_bf16_f32 v68, v96, v97\n\t"
        "v_mfma_f32_16x16x32_bf16 v[140:143], v[44:47], v[60:63], v[140:143]\n\t"
        "ds_read_b64_tr_b16 v[42:43], %[wa0] offset:6976\n\t"
        "v_cvt_pk_bf16_f32 v69, v98, v99\n\t"
        "v_mfma_f32_16x16x32_bf16 v[116:119], v[44:47], v[56:59], v[116:119]\n\t"
        "ds_read_b64_tr_b16 v[44:45], %[wa1] offset:6208\n\t"
        "v_cvt_pk_bf16_f32 v70, v100, v101\n\t"
        "s_waitcnt lgkmcnt(14)\n\t"
        "v_mfma_f32_16x16x32_bf16 v[120:123], v[48:51], v[56:59], v[120:123]\n\t"
        "ds_read_b64_tr_b16 v[46:47], %[wa1] offset:6976\n\t"
        "v_cvt_pk_bf16_f32 v71, v102, v103\n\t"
        "v_mfma_f32_16x16x32_bf16 v[144:147], v[48:51], v[60:63], v[144:147]\n\t"
        "ds_read_b64_tr_b16 v[48:49], %[wa0] offset:6272\n\t"
        "v_mfma_f32_16x16x32_bf16 v[148:151], v[52:55], v[60:63], v[148:151]\n\t"
        "ds_read_b64_tr_b16 v[50:51], %[wa0] offset:7040\n\t"
        "s_sub_u32 %[tn], %[tn], 1\n\t"
        "v_mfma_f32_16x16x32_bf16 v[124:127], v[52:55], v[56:59], v[124:127]\n\t"
        "ds_read_b64_tr_b16 v[52:53], %[wa1] offset:6272\n\t"
        "ds_read_b64_tr_b16 v[54:55], %[wa1] offset:7040\n\t"
        "s_cbranch_scc1 9f\n\t"
        "s_waitcnt lgkmcnt(14)\n\t"
        "v_mfma_f32_16x16x32_bf16 v[88:91], v[72:75], v[24:27], 0\n\t"
        "ds_read_b64_tr_b16 v[72:73], v20 offset:128\n\t"
        "ds_read_b64_tr_b16 v[74:75], v21 offset:128\n\t"
        "v_mfma_f32_16x16x32_bf16 v[92:95], v[76:79], v[24:27], 0\n\t"
        "ds_read_b64_tr_b16 v[76:77], v20 offset:136\n\t"
        "ds_read_b64_tr_b16 v[78:79], v21 offset:136\n\t"
        "s_waitcnt lgkmcnt(14)\n\t"
        "v_mfma_f32_16x16x32_bf16 v[96:99], v[80:83], v[28:31], 0\n\t"
        "ds_read_b64_tr_b16 v[80:81], v22 offset:128\n\t"
        "ds_read_b64_tr_b16 v[82:83], v23 offset:128\n\t"
        "v_mfma_f32_16x16x32_bf16 v[100:103], v[84:87], v[28:31], 0\n\t"
        "ds_read_b64_tr_b16 v[84:85], v22 offset:136\n\t"
        "ds_read_b64_tr_b16 v[86:87], v23 offset:136\n\t"
        "s_waitcnt lgkmcnt(14)\n\t"
        "v_mfma_f32_16x16x32_bf16 v[104:107], v[32:35], v[64:67], v[104:107]\n\t"
        "ds_read_b128 v[16:19], %[ta] offset:0\n\t"
        "v_mfma_f32_16x16x32_bf16 v[128:131], v[32:35], v[68:71], v[128:131]\n\t"
        "ds_read_b64_tr_b16 v[32:33], %[wa0] offset:12288\n\t"
        "v_cvt_pk_bf16_f32 v56, v88, v89\n\t"
        "v_cvt_pk_bf16_f32 v57, v90, v91\n\t"
        "v_mfma_f32_16x16x32_bf16 v[132:135], v[36:39], v[68:71], v[132:135]\n\t"
        "ds_read_b64_tr_b16 v[34:35], %[wa0] offset:13056\n\t"
        "v_cvt_pk_bf16_f32 v58, v92, v93\n\t"
        "v_cvt_pk_bf16_f32 v59, v94, v95\n\t"
        "v_mfma_f32_16x16x32_bf16 v[108:111], v[36:39], v[64:67], v[108:111]\n\t"
        "ds_read_b64_tr_b16 v[36:37], %[wa1] offset:12288\n\t"
        "v_cvt_pk_bf16_f32 v60, v96, v97\n\t"
        "v_cvt_pk_bf16_f32 v61, v98, v99\n\t"
        "s_waitcnt lgkmcnt(14)\n\t"
        "v_mfma_f32_16x16x32_bf16 v[112:115], v[40:43], v[64:67], v[112:115]\n\t"
        "ds_read_b64_tr_b16 v[38:39], %[wa1] offset:13056\n\t"
        "v_cvt_pk_bf16_f32 v62, v100, v101\n\t"
        "v_cvt_pk_bf16_f32 v63, v102, v103\n\t"
        "v_mfma_f32_16x16x32_bf16 v[136:139], v[40:43], v[68:71], v[136:139]\n\t"
        "ds_read_b64_tr_b16 v[40:41], %[wa0] offset:12352\n\t"
        "v_mfma_f32_16x16x32_bf16 v[140:143], v[44:47], v[68:71], v[140:143]\n\t"
        "ds_read_b64_tr_b16 v[42:43], %[wa0] offset:13120\n\t"
        "s_waitcnt lgkmcnt(6)\n\t"
        "v_mfma_f32_16x16x32_bf16 v[116:119], v[44:47], v[64:67], v[116:119]\n\t"
        "ds_read_b64_tr_b16 v[44:45], %[wa1] offset:12352\n\t"
        "v_add_u32 v14, v14, v15\n\t"
        "v_mfma_f32_16x16x32_bf16 v[120:123], v[48:51], v[64:67], v[120:123]\n\t"
        "ds_read_b64_tr_b16 v[46:47], %[wa1] offset:13120\n\t"
        "v_cmp_lt_u16 vcc, 208, v14\n\t"
        "v_mfma_f32_16x16x32_bf16 v[144:147], v[48:51], v[68:71], v[144:147]\n\t"
        "ds_read_b64_tr_b16 v[48:49], %[wa0] offset:12416\n\t"
        "v_mov_b32 v13, v15\n\t"
        "v_mfma_f32_16x16x32_bf16 v[148:151], v[52:55], v[68:71], v[148:151]\n\t"
        "ds_read_b64_tr_b16 v[50:51], %[wa0] offset:13184\n\t"
        "v_add_u32 %[ca], 8, %[ca]\n\t"
        "v_mfma_f32_16x16x32_bf16 v[124:127], v[52:55], v[64:67], v[124:127]\n\t"
        "ds_read_b64_tr_b16 v[52:53], %[wa1] offset:12416\n\t"
        "ds_read_b64_tr_b16 v[54:55], %[wa1] offset:13184\n\t"
        "v_mfma_f32_16x16x32_bf16 v[88:91], v[72:75], v[24:27], 0\n\t"
        "v_xor_b32 v16, %[cx], v16\n\t"
        "v_mad_u32_u16 v20, v16, 16, 0\n\t"
        "v_mfma_f32_16x16x32_bf16 v[92:95], v[76:79], v[24:27], 0\n\t"
        "v_mad_u32_u16 v21, v16, 16, 0 op_sel:[1,0,0,0]\n\t"
        "v_xor_b32 v18, %[cx], v18\n\t"
        "v_perm_b32 v24, v17, v17, %[sel0]\n\t"
        "v_mfma_f32_16x16x32_bf16 v[96:99], v[80:83], v[28:31], 0\n\t"
        "v_mad_u32_u16 v22, v18, 16, 0\n\t"
        "v_mad_u32_u16 v23, v18, 16, 0 op_sel:[1,0,0,0]\n\t"
        "v_perm_b32 v25, v17, v17, %[sel1]\n\t"
        "v_mfma_f32_16x16x32_bf16 v[100:103], v[84:87], v[28:31], 0\n\t"
        "s_waitcnt vmcnt(0) lgkmcnt(0)\n\t"
        "s_barrier\n\t"
        "s_cbranch_vccnz 211f\n\t"
        "212:\n\t"
        "v_mfma_f32_16x16x32_bf16 v[104:107], v[32:35], v[56:59], v[104:107]\n\t"
        "ds_read_b64_tr_b16 v[72:73], v20 offset:0\n\t"
        "ds_read_b64_tr_b16 v[74:75], v21 offset:0\n\t"
        "v_perm_b32 v26, v17, v17, %[sel2]\n\t"
        "v_mfma_f32_16x16x32_bf16 v[128:131], v[32:35], v[60:63], v[128:131]\n\t"
        "ds_read_b64_tr_b16 v[76:77], v20 offset:8\n\t"
        "ds_read_b64_tr_b16 v[78:79], v21 offset:8\n\t"
        "v_cvt_pk_bf16_f32 v64, v88, v89\n\t"
        "v_mfma_f32_16x16x32_bf16 v[132:135], v[36:39], v[60:63], v[132:135]\n\t"
        "ds_read_b64_tr_b16 v[80:81], v22 offset:0\n\t"
        "ds_read_b64_tr_b16 v[82:83], v23 offset:0\n\t"
        "v_cvt_pk_bf16_f32 v65, v90, v91\n\t"
        "v_mfma_f32_16x16x32_bf16 v[108:111], v[36:39], v[56:59], v[108:111]\n\t"
        "ds_read_b64_tr_b16 v[84:85], v22 offset:8\n\t"
        "ds_read_b64_tr_b16 v[86:87], v23 offset:8\n\t"
        "v_cvt_pk_bf16_f32 v66, v92, v93\n\t"
        "v_mfma_f32_16x16x32_bf16 v[112:115], v[40:43], v[56:59], v[112:115]\n\t"
        "ds_read_b64_tr_b16 v[32:33], %[wa0] offset:18432\n\t"
        "ds_read_b64_tr_b16 v[34:35], %[wa0] offset:19200\n\t"
        "v_cvt_pk_bf16_f32 v67, v94, v95\n\t"
        "v_mfma_f32_16x16x32_bf16 v[136:139], v[40:43], v[60:63], v[136:139]\n\t"
        "ds_read_b64_tr_b16 v[36:37], %[wa1] offset:18432\n\t"
        "ds_read_b64_tr_b16 v[38:39], %[wa1] offset:19200\n\t"
        "v_cvt_pk_bf16_f32 v68, v96, v97\n\t"
        "v_mfma_f32_16x16x32_bf16 v[140:143], v[44:47], v[60:63], v[140:143]\n\t"
        "ds_read_b64_tr_b16 v[40:41], %[wa0] offset:18496\n\t"
        "ds_read_b64_tr_b16 v[42:43], %[wa0] offset:19264\n\t"
        "v_cvt_pk_bf16_f32 v69, v98, v99\n\t"
        "v_mfma_f32_16x16x32_bf16 v[116:119], v[44:47], v[56:59], v[116:119]\n\t"
        "ds_read_b64_tr_b16 v[44:45], %[wa1] offset:18496\n\t"
        "ds_read_b64_tr_b16 v[46:47], %[wa1] offset:19264\n\t"
        "v_cvt_pk_bf16_f32 v70, v100, v101\n\t"
        "v_mfma_f32_16x16x32_bf16 v[120:123], v[48:51], v[56:59], v[120:123]\n\t"
        "v_cvt_pk_bf16_f32 v71, v102, v103\n\t"
        "v_perm_b32 v27, v17, v17, %[sel3]\n\t"
        "v_perm_b32 v28, v19, v19, %[sel0]\n\t"
        "v_mfma_f32_16x16x32_bf16 v[144:147], v[48:51], v[60:63], v[144:147]\n\t"
        "ds_read_b64_tr_b16 v[48:49], %[wa0] offset:18560\n\t"
        "ds_read_b64_tr_b16 v[50:51], %[wa0] offset:19328\n\t"
        "v_perm_b32 v29, v19, v19, %[sel1]\n\t"
        "v_mfma_f32_16x16x32_bf16 v[148:151], v[52:55], v[60:63], v[148:151]\n\t"
        "v_perm_b32 v30, v19, v19, %[sel2]\n\t"
        "v_perm_b32 v31, v19, v19, %[sel3]\n\t"
        "v_mfma_f32_16x16x32_bf16 v[124:127], v[52:55], v[56:59], v[124:127]\n\t"
        "ds_read_b64_tr_b16 v[52:53], %[wa1] offset:18560\n\t"
        "ds_read_b64_tr_b16 v[54:55], %[wa1] offset:19328\n\t"
        "s_waitcnt lgkmcnt(14)\n\t"
        "v_mfma_f32_16x16x32_bf16 v[88:91], v[72:75], v[24:27], 0\n\t"
        "ds_read_b64_tr_b16 v[72:73], v20 offset:64\n\t"
        "ds_read_b64_tr_b16 v[74:75], v21 offset:64\n\t"
        "v_mfma_f32_16x16x32_bf16 v[92:95], v[76:79], v[24:27], 0\n\t"
        "ds_read_b64_tr_b16 v[76:77], v20 offset:72\n\t"
        "ds_read_b64_tr_b16 v[78:79], v21 offset:72\n\t"
        "s_waitcnt lgkmcnt(14)\n\t"
        "v_mfma_f32_16x16x32_bf16 v[96:99], v[80:83], v[28:31], 0\n\t"
        "ds_read_b64_tr_b16 v[80:81], v22 offset:64\n\t"
        "ds_read_b64_tr_b16 v[82:83], v23 offset:64\n\t"
        "v_mfma_f32_16x16x32_bf16 v[100:103], v[84:87], v[28:31], 0\n\t"
        "ds_read_b64_tr_b16 v[84:85], v22 offset:72\n\t"
        "ds_read_b64_tr_b16 v[86:87], v23 offset:72\n\t"
        "s_waitcnt lgkmcnt(14)\n\t"
        "v_mfma_f32_16x16x32_bf16 v[104:107], v[32:35], v[64:67], v[104:107]\n\t"
        "v_mfma_f32_16x16x32_bf16 v[128:131], v[32:35], v[68:71], v[128:131]\n\t"
        "ds_read_b64_tr_b16 v[32:33], %[wa0] offset:24576\n\t"
        "v_cvt_pk_bf16_f32 v56, v88, v89\n\t"
        "v_mfma_f32_16x16x32_bf16 v[132:135], v[36:39], v[68:71], v[132:135]\n\t"
        "ds_read_b64_tr_b16 v[34:35], %[wa0] offset:25344\n\t"
        "v_cvt_pk_bf16_f32 v57, v90, v91\n\t"
        "v_mfma_f32_16x16x32_bf16 v[108:111], v[36:39], v[64:67], v[108:111]\n\t"
        "ds_read_b64_tr_b16 v[36:37], %[wa1] offset:24576\n\t"
        "v_cvt_pk_bf16_f32 v58, v92, v93\n\t"
        "s_waitcnt lgkmcnt(14)\n\t"
        "v_mfma_f32_16x16x32_bf16 v[112:115], v[40:43], v[64:67], v[112:115]\n\t"
        "ds_read_b64_tr_b16 v[38:39], %[wa1] offset:25344\n\t"
        "v_cvt_pk_bf16_f32 v59, v94, v95\n\t"
        "v_mfma_f32_16x16x32_bf16 v[136:139], v[40:43], v[68:71], v[136:139]\n\t"
        "ds_read_b64_tr_b16 v[40:41], %[wa0] offset:24640\n\t"
        "v_cvt_pk_bf16_f32 v60, v96, v97\n\t"
        "v_mfma_f32_16x16x32_bf16 v[140:143], v[44:47], v[68:71], v[140:143]\n\t"
        "ds_read_b64_tr_b16 v[42:43], %[wa0] offset:25408\n\t"
        "v_cvt_pk_bf16_f32 v61, v98, v99\n\t"
        "v_mfma_f32_16x16x32_bf16 v[116:119], v[44:47], v[64:67], v[116:119]\n\t"
        "ds_read_b64_tr_b16 v[44:45], %[wa1] offset:24640\n\t"
        "v_cvt_pk_bf16_f32 v62, v100, v101\n\t"
        "s_waitcnt lgkmcnt(14)\n\t"
        "v_mfma_f32_16x16x32_bf16 v[120:123], v[48:51], v[64:67], v[120:123]\n\t"
        "ds_read_b64_tr_b16 v[46:47], %[wa1] offset:25408\n\t"
        "v_cvt_pk_bf16_f32 v63, v102, v103\n\t"
        "v_mfma_f32_16x16x32_bf16 v[144:147], v[48:51], v[68:71], v[144:147]\n\t"
        "ds_read_b64_tr_b16 v[48:49], %[wa0] offset:24704\n\t"
        "v_mfma_f32_16x16x32_bf16 v[148:151], v[52:55], v[68:71], v[148:151]\n\t"
        "ds_read_b64_tr_b16 v[50:51], %[wa0] offset:25472\n\t"
        "v_mfma_f32_16x16x32_bf16 v[124:127], v[52:55], v[64:67], v[124:127]\n\t"
        "ds_read_b64_tr_b16 v[52:53], %[wa1] offset:24704\n\t"
        "ds_read_b64_tr_b16 v[54:55], %[wa1] offset:25472\n\t"
        "s_branch 20b\n\t"
        "201:\n\t"
        "s_barrier\n\t"
        "s_branch 202b\n\t"
        "211:\n\t"
        "s_barrier\n\t"
        "s_branch 212b\n\t"
        "15:\n\t"
        "s_setprio 0\n\t"
        "ds_read_b64_tr_b16 v[72:73], v20 offset:0\n\t"
        "ds_read_b64_tr_b16 v[74:75], v21 offset:0\n\t"
        "ds_read_b64_tr_b16 v[76:77], v20 offset:8\n\t"
        "ds_read_b64_tr_b16 v[78:79], v21 offset:8\n\t"
        "s_waitcnt lgkmcnt(0)\n\t"
        "v_mfma_f32_16x16x32_bf16 v[88:91], v[72:75], v[24:27], 0\n\t"
        "v_mfma_f32_16x16x32_bf16 v[92:95], v[76:79], v[24:27], 0\n\t"
        "ds_read_b64_tr_b16 v[72:73], v20 offset:64\n\t"
        "ds_read_b64_tr_b16 v[74:75], v21 offset:64\n\t"
        "ds_read_b64_tr_b16 v[76:77], v20 offset:72\n\t"
        "ds_read_b64_tr_b16 v[78:79], v21 offset:72\n\t"
        "ds_read2_b32 v[14:15], %[ca] offset0:1 offset1:2\n\t"
        "ds_read_b128 v[16:19], %[ta] offset:1024\n\t"
        "s_nop 5\n\t"
        "v_cvt_pk_bf16_f32 v56, v88, v89\n\t"
        "v_cvt_pk_bf16_f32 v57, v90, v91\n\t"
        "v_cvt_pk_bf16_f32 v58, v92, v93\n\t"
        "v_cvt_pk_bf16_f32 v59, v94, v95\n\t"
        "s_waitcnt lgkmcnt(0)\n\t"
        ".p2align 6\n\t"
        "10:\n\t"
        "s_waitcnt lgkmcnt(12)\n\t"
        "v_mfma_f32_16x16x32_bf16 v[88:91], v[72:75], v[24:27], 0\n\t"
        "ds_read_b64_tr_b16 v[72:73], v20 offset:128\n\t"
        "ds_read_b64_tr_b16 v[74:75], v21 offset:128\n\t"
        "ds_read2_b32 v[14:15], %[ca] offset0:1 offset1:2\n\t"
        "v_mfma_f32_16x16x32_bf16 v[92:95], v[76:79], v[24:27], 0\n\t"
        "ds_read_b64_tr_b16 v[76:77], v20 offset:136\n\t"
        "ds_read_b64_tr_b16 v[78:79], v21 offset:136\n\t"
        "ds_read_b128 v[16:19], %[ta] offset:1024\n\t"
        "s_waitcnt lgkmcnt(14)\n\t"
        "v_mfma_f32_16x16x32_bf16 v[104:107], v[32:35], v[56:59], v[104:107]\n\t"
        "ds_read_b64_tr_b16 v[32:33], %[wa0] offset:30720\n\t"
        "ds_read_b64_tr_b16 v[34:35], %[wa0] offset:31488\n\t"
        "v_mfma_f32_16x16x32_bf16 v[108:111], v[36:39], v[56:59], v[108:111]\n\t"
        "ds_read_b64_tr_b16 v[36:37], %[wa1] offset:30720\n\t"
        "ds_read_b64_tr_b16 v[38:39], %[wa1] offset:31488\n\t"
        "s_waitcnt lgkmcnt(14)\n\t"
        "v_mfma_f32_16x16x32_bf16 v[112:115], v[40:43], v[56:59], v[112:115]\n\t"
        "ds_read_b64_tr_b16 v[40:41], %[wa0] offset:30784\n\t"
        "ds_read_b64_tr_b16 v[42:43], %[wa0] offset:31552\n\t"
        "s_waitcnt lgkmcnt(6)\n\t"
        "v_cvt_pk_bf16_f32 v64, v88, v89\n\t"
        "v_mfma_f32_16x16x32_bf16 v[116:119], v[44:47], v[56:59], v[116:119]\n\t"
        "ds_read_b64_tr_b16 v[44:45], %[wa1] offset:30784\n\t"
        "ds_read_b64_tr_b16 v[46:47], %[wa1] offset:31552\n\t"
        "v_add_u32 v13, v13, v14\n\t"
        "v_cvt_pk_bf16_f32 v65, v90, v91\n\t"
        "v_mfma_f32_16x16x32_bf16 v[120:123], v[48:51], v[56:59], v[120:123]\n\t"
        "ds_read_b64_tr_b16 v[48:49], %[wa0] offset:30848\n\t"
        "ds_read_b64_tr_b16 v[50:51], %[wa0] offset:31616\n\t"
        "v_cmp_lt_u16 vcc, 208, v13\n\t"
        "v_cvt_pk_bf16_f32 v66, v92, v93\n\t"
        "v_mfma_f32_16x16x32_bf16 v[124:127], v[52:55], v[56:59], v[124:127]\n\t"
        "ds_read_b64_tr_b16 v[52:53], %[wa1] offset:30848\n\t"
        "ds_read_b64_tr_b16 v[54:55], %[wa1] offset:31616\n\t"
        "v_xor_b32 v16, %[cx], v16\n\t"
        "v_cvt_pk_bf16_f32 v67, v94, v95\n\t"
        "v_mfma_f32_16x16x32_bf16 v[88:91], v[72:75], v[24:27], 0\n\t"
        "v_mad_u32_u16 v20, v16, 16, 0\n\t"
        "v_mad_u32_u16 v21, v16, 16, 0 op_sel:[1,0,0,0]\n\t"
        "v_mfma_f32_16x16x32_bf16 v[92:95], v[76:79], v[24:27], 0\n\t"
        "s_waitcnt lgkmcnt(0)\n\t"
        "s_barrier\n\t"
        "s_cbranch_vccnz 101f\n\t"
        "102:\n\t"
        "v_mfma_f32_16x16x32_bf16 v[104:107], v[32:35], v[64:67], v[104:107]\n\t"
        "ds_read_b64_tr_b16 v[72:73], v20 offset:0\n\t"
        "ds_read_b64_tr_b16 v[74:75], v21 offset:0\n\t"
        "ds_read_b64_tr_b16 v[76:77], v20 offset:8\n\t"
        "v_perm_b32 v24, v17, v17, %[sel0]\n\t"
        "v_perm_b32 v25, v17, v17, %[sel1]\n\t"
        "v_mfma_f32_16x16x32_bf16 v[108:111], v[36:39], v[64:67], v[108:111]\n\t"
        "ds_read_b64_tr_b16 v[78:79], v21 offset:8\n\t"
        "ds_read_b64_tr_b16 v[32:33], %[wa0] offset:0\n\t"
        "ds_read_b64_tr_b16 v[34:35], %[wa0] offset:768\n\t"
        "v_perm_b32 v26, v17, v17, %[sel2]\n\t"
        "v_perm_b32 v27, v17, v17, %[sel3]\n\t"
        "v_mfma_f32_16x16x32_bf16 v[112:115], v[40:43], v[64:67], v[112:115]\n\t"
        "ds_read_b64_tr_b16 v[36:37], %[wa1] offset:0\n\t"
        "ds_read_b64_tr_b16 v[38:39], %[wa1] offset:768\n\t"
        "ds_read_b64_tr_b16 v[40:41], %[wa0] offset:64\n\t"
        "v_cvt_pk_bf16_f32 v56, v88, v89\n\t"
        "v_mfma_f32_16x16x32_bf16 v[116:119], v[44:47], v[64:67], v[116:119]\n\t"
        "ds_read_b64_tr_b16 v[42:43], %[wa0] offset:832\n\t"
        "ds_read_b64_tr_b16 v[44:45], %[wa1] offset:64\n\t"
        "ds_read_b64_tr_b16 v[46:47], %[wa1] offset:832\n\t"
        "v_cvt_pk_bf16_f32 v57, v90, v91\n\t"
        "v_mfma_f32_16x16x32_bf16 v[120:123], v[48:51], v[64:67], v[120:123]\n\t"
        "ds_read_b64_tr_b16 v[48:49], %[wa0] offset:128\n\t"
        "ds_read_b64_tr_b16 v[50:51], %[wa0] offset:896\n\t"
        "v_cvt_pk_bf16_f32 v58, v92, v93\n\t"
        "v_cvt_pk_bf16_f32 v59, v94, v95\n\t"
        "v_mfma_f32_16x16x32_bf16 v[124:127], v[52:55], v[64:67], v[124:127]\n\t"
        "ds_read_b64_tr_b16 v[52:53], %[wa1] offset:128\n\t"
        "ds_read_b64_tr_b16 v[54:55], %[wa1] offset:896\n\t"
        "s_waitcnt lgkmcnt(12)\n\t"
        "v_mfma_f32_16x16x32_bf16 v[88:91], v[72:75], v[24:27], 0\n\t"
        "ds_read_b64_tr_b16 v[72:73], v20 offset:64\n\t"
        "ds_read_b64_tr_b16 v[74:75], v21 offset:64\n\t"
        "v_mfma_f32_16x16x32_bf16 v[92:95], v[76:79], v[24:27], 0\n\t"
        "ds_read_b64_tr_b16 v[76:77], v20 offset:72\n\t"
        "ds_read_b64_tr_b16 v[78:79], v21 offset:72\n\t"
        "s_waitcnt lgkmcnt(14)\n\t"
        "v_mfma_f32_16x16x32_bf16 v[104:107], v[32:35], v[56:59], v[104:107]\n\t"
        "ds_read_b64_tr_b16 v[32:33], %[wa0] offset:6144\n\t"
        "ds_read_b64_tr_b16 v[34:35], %[wa0] offset:6912\n\t"
        "s_waitcnt lgkmcnt(14)\n\t"
        "v_mfma_f32_16x16x32_bf16 v[108:111], v[36:39], v[56:59], v[108:111]\n\t"
        "ds_read_b64_tr_b16 v[36:37], %[wa1] offset:6144\n\t"
        "ds_read_b64_tr_b16 v[38:39], %[wa1] offset:6912\n\t"
        "s_waitcnt lgkmcnt(14)\n\t"
        "v_mfma_f32_16x16x32_bf16 v[112:115], v[40:43], v[56:59], v[112:115]\n\t"
        "ds_read_b64_tr_b16 v[40:41], %[wa0] offset:6208\n\t"
        "ds_read_b64_tr_b16 v[42:43], %[wa0] offset:6976\n\t"
        "v_cvt_pk_bf16_f32 v64, v88, v89\n\t"
        "s_waitcnt lgkmcnt(14)\n\t"
        "v_mfma_f32_16x16x32_bf16 v[116:119], v[44:47], v[56:59], v[116:119]\n\t"
        "ds_read_b64_tr_b16 v[44:45], %[wa1] offset:6208\n\t"
        "ds_read_b64_tr_b16 v[46:47], %[wa1] offset:6976\n\t"
        "v_cvt_pk_bf16_f32 v65, v90, v91\n\t"
        "s_waitcnt lgkmcnt(14)\n\t"
        "v_mfma_f32_16x16x32_bf16 v[120:123], v[48:51], v[56:59], v[120:123]\n\t"
        "ds_read_b64_tr_b16 v[48:49], %[wa0] offset:6272\n\t"
        "ds_read_b64_tr_b16 v[50:51], %[wa0] offset:7040\n\t"
        "v_cvt_pk_bf16_f32 v66, v92, v93\n\t"
        "s_sub_u32 %[tn], %[tn], 1\n\t"
        "s_waitcnt lgkmcnt(14)\n\t"
        "v_mfma_f32_16x16x32_bf16 v[124:127], v[52:55], v[56:59], v[124:127]\n\t"
        "ds_read_b64_tr_b16 v[52:53], %[wa1] offset:6272\n\t"
        "ds_read_b64_tr_b16 v[54:55], %[wa1] offset:7040\n\t"
        "v_cvt_pk_bf16_f32 v67, v94, v95\n\t"
        "s_cbranch_scc1 9f\n\t"
        "s_waitcnt lgkmcnt(12)\n\t"
        "v_mfma_f32_16x16x32_bf16 v[88:91], v[72:75], v[24:27], 0\n\t"
        "ds_read_b64_tr_b16 v[72:73], v20 offset:128\n\t"
        "ds_read_b64_tr_b16 v[74:75], v21 offset:128\n\t"
        "ds_read_b128 v[16:19], %[ta] offset:0\n\t"
        "v_mfma_f32_16x16x32_bf16 v[92:95], v[76:79], v[24:27], 0\n\t"
        "ds_read_b64_tr_b16 v[76:77], v20 offset:136\n\t"
        "ds_read_b64_tr_b16 v[78:79], v21 offset:136\n\t"
        "s_waitcnt lgkmcnt(14)\n\t"
        "v_mfma_f32_16x16x32_bf16 v[104:107], v[32:35], v[64:67], v[104:107]\n\t"
        "ds_read_b64_tr_b16 v[32:33], %[wa0] offset:12288\n\t"
        "ds_read_b64_tr_b16 v[34:35], %[wa0] offset:13056\n\t"
        "s_waitcnt lgkmcnt(14)\n\t"
        "v_mfma_f32_16x16x32_bf16 v[108:111], v[36:39], v[64:67], v[108:111]\n\t"
        "ds_read_b64_tr_b16 v[36:37], %[wa1] offset:12288\n\t"
        "ds_read_b64_tr_b16 v[38:39], %[wa1] offset:13056\n\t"
        "s_waitcnt lgkmcnt(6)\n\t"
        "v_mfma_f32_16x16x32_bf16 v[112:115], v[40:43], v[64:67], v[112:115]\n\t"
        "ds_read_b64_tr_b16 v[40:41], %[wa0] offset:12352\n\t"
        "ds_read_b64_tr_b16 v[42:43], %[wa0] offset:13120\n\t"
        "v_add_u32 v14, v14, v15\n\t"
        "v_cvt_pk_bf16_f32 v56, v88, v89\n\t"
        "v_mfma_f32_16x16x32_bf16 v[116:119], v[44:47], v[64:67], v[116:119]\n\t"
        "ds_read_b64_tr_b16 v[44:45], %[wa1] offset:12352\n\t"
        "ds_read_b64_tr_b16 v[46:47], %[wa1] offset:13120\n\t"
        "v_cmp_lt_u16 vcc, 208, v14\n\t"
        "v_cvt_pk_bf16_f32 v57, v90, v91\n\t"
        "v_mfma_f32_16x16x32_bf16 v[120:123], v[48:51], v[64:67], v[120:123]\n\t"
        "ds_read_b64_tr_b16 v[48:49], %[wa0] offset:12416\n\t"
        "ds_read_b64_tr_b16 v[50:51], %[wa0] offset:13184\n\t"
        "v_mov_b32 v13, v15\n\t"
        "v_cvt_pk_bf16_f32 v58, v92, v93\n\t"
        "v_mfma_f32_16x16x32_bf16 v[124:127], v[52:55], v[64:67], v[124:127]\n\t"
        "ds_read_b64_tr_b16 v[52:53], %[wa1] offset:12416\n\t"
        "ds_read_b64_tr_b16 v[54:55], %[wa1] offset:13184\n\t"
        "v_add_u32 %[ca], 8, %[ca]\n\t"
        "v_cvt_pk_bf16_f32 v59, v94, v95\n\t"
        "s_waitcnt lgkmcnt(12)\n\t"
        "v_mfma_f32_16x16x32_bf16 v[88:91], v[72:75], v[24:27], 0\n\t"
        "v_xor_b32 v16, %[cx], v16\n\t"
        "v_mad_u32_u16 v20, v16, 16, 0\n\t"
        "v_mad_u32_u16 v21, v16, 16, 0 op_sel:[1,0,0,0]\n\t"
        "v_mfma_f32_16x16x32_bf16 v[92:95], v[76:79], v[24:27], 0\n\t"
        "s_waitcnt vmcnt(0) lgkmcnt(0)\n\t"
        "s_barrier\n\t"
        "s_cbranch_vccnz 111f\n\t"
        "112:\n\t"
        "v_mfma_f32_16x16x32_bf16 v[104:107], v[32:35], v[56:59], v[104:107]\n\t"
        "ds_read_b64_tr_b16 v[72:73], v20 offset:0\n\t"
        "ds_read_b64_tr_b16 v[74:75], v21 offset:0\n\t"
        "ds_read_b64_tr_b16 v[76:77], v20 offset:8\n\t"
        "v_perm_b32 v24, v17, v17, %[sel0]\n\t"
        "v_perm_b32 v25, v17, v17, %[sel1]\n\t"
        "v_mfma_f32_16x16x32_bf16 v[108:111], v[36:39], v[56:59], v[108:111]\n\t"
        "ds_read_b64_tr_b16 v[78:79], v21 offset:8\n\t"
        "ds_read_b64_tr_b16 v[32:33], %[wa0] offset:18432\n\t"
        "ds_read_b64_tr_b16 v[34:35], %[wa0] offset:19200\n\t"
        "v_perm_b32 v26, v17, v17, %[sel2]\n\t"
        "v_perm_b32 v27, v17, v17, %[sel3]\n\t"
        "v_mfma_f32_16x16x32_bf16 v[112:115], v[40:43], v[56:59], v[112:115]\n\t"
        "ds_read_b64_tr_b16 v[36:37], %[wa1] offset:18432\n\t"
        "ds_read_b64_tr_b16 v[38:39], %[wa1] offset:19200\n\t"
        "ds_read_b64_tr_b16 v[40:41], %[wa0] offset:18496\n\t"
        "v_cvt_pk_bf16_f32 v64, v88, v89\n\t"
        "v_mfma_f32_16x16x32_bf16 v[116:119], v[44:47], v[56:59], v[116:119]\n\t"
        "ds_read_b64_tr_b16 v[42:43], %[wa0] offset:19264\n\t"
        "ds_read_b64_tr_b16 v[44:45], %[wa1] offset:18496\n\t"
        "ds_read_b64_tr_b16 v[46:47], %[wa1] offset:19264\n\t"
        "v_cvt_pk_bf16_f32 v65, v90, v91\n\t"
        "v_mfma_f32_16x16x32_bf16 v[120:123], v[48:51], v[56:59], v[120:123]\n\t"
        "ds_read_b64_tr_b16 v[48:49], %[wa0] offset:18560\n\t"
        "ds_read_b64_tr_b16 v[50:51], %[wa0] offset:19328\n\t"
        "v_cvt_pk_bf16_f32 v66, v92, v93\n\t"
        "v_cvt_pk_bf16_f32 v67, v94, v95\n\t"
        "v_mfma_f32_16x16x32_bf16 v[124:127], v[52:55], v[56:59], v[124:127]\n\t"
        "ds_read_b64_tr_b16 v[52:53], %[wa1] offset:18560\n\t"
        "ds_read_b64_tr_b16 v[54:55], %[wa1] offset:19328\n\t"
        "s_waitcnt lgkmcnt(12)\n\t"
        "v_mfma_f32_16x16x32_bf16 v[88:91], v[72:75], v[24:27], 0\n\t"
        "ds_read_b64_tr_b16 v[72:73], v20 offset:64\n\t"
        "ds_read_b64_tr_b16 v[74:75], v21 offset:64\n\t"
        "v_mfma_f32_16x16x32_bf16 v[92:95], v[76:79], v[24:27], 0\n\t"
        "ds_read_b64_tr_b16 v[76:77], v20 offset:72\n\t"
        "ds_read_b64_tr_b16 v[78:79], v21 offset:72\n\t"
        "s_waitcnt lgkmcnt(14)\n\t"
        "v_mfma_f32_16x16x32_bf16 v[104:107], v[32:35], v[64:67], v[104:107]\n\t"
        "ds_read_b64_tr_b16 v[32:33], %[wa0] offset:24576\n\t"
        "ds_read_b64_tr_b16 v[34:35], %[wa0] offset:25344\n\t"
        "s_waitcnt lgkmcnt(14)\n\t"
        "v_mfma_f32_16x16x32_bf16 v[108:111], v[36:39], v[64:67], v[108:111]\n\t"
        "ds_read_b64_tr_b16 v[36:37], %[wa1] offset:24576\n\t"
        "ds_read_b64_tr_b16 v[38:39], %[wa1] offset:25344\n\t"
        "s_waitcnt lgkmcnt(14)\n\t"
        "v_mfma_f32_16x16x32_bf16 v[112:115], v[40:43], v[64:67], v[112:115]\n\t"
        "ds_read_b64_tr_b16 v[40:41], %[wa0] offset:24640\n\t"
        "ds_read_b64_tr_b16 v[42:43], %[wa0] offset:25408\n\t"
        "v_cvt_pk_bf16_f32 v56, v88, v89\n\t"
        "s_waitcnt lgkmcnt(14)\n\t"
        "v_mfma_f32_16x16x32_bf16 v[116:119], v[44:47], v[64:67], v[116:119]\n\t"
        "ds_read_b64_tr_b16 v[44:45], %[wa1] offset:24640\n\t"
        "ds_read_b64_tr_b16 v[46:47], %[wa1] offset:25408\n\t"
        "v_cvt_pk_bf16_f32 v57, v90, v91\n\t"
        "s_waitcnt lgkmcnt(14)\n\t"
        "v_mfma_f32_16x16x32_bf16 v[120:123], v[48:51], v[64:67], v[120:123]\n\t"
        "ds_read_b64_tr_b16 v[48:49], %[wa0] offset:24704\n\t"
        "ds_read_b64_tr_b16 v[50:51], %[wa0] offset:25472\n\t"
        "v_cvt_pk_bf16_f32 v58, v92, v93\n\t"
        "s_waitcnt lgkmcnt(14)\n\t"
        "v_mfma_f32_16x16x32_bf16 v[124:127], v[52:55], v[64:67], v[124:127]\n\t"
        "ds_read_b64_tr_b16 v[52:53], %[wa1] offset:24704\n\t"
        "ds_read_b64_tr_b16 v[54:55], %[wa1] offset:25472\n\t"
        "v_cvt_pk_bf16_f32 v59, v94, v95\n\t"
        "s_branch 10b\n\t"
        "101:\n\t"
        "s_barrier\n\t"
        "s_branch 102b\n\t"
        "111:\n\t"
        "s_barrier\n\t"
        "s_branch 112b\n\t"
        "9:\n\t"
        "s_waitcnt lgkmcnt(0)\n\t"
        "s_nop 7\n\t"
        "v_cvt_pk_bf16_f32 v32, v104, v105\n\t"
        "v_cvt_pk_bf16_f32 v33, v106, v107\n\t"
        "v_cvt_pk_bf16_f32 v34, v108, v109\n\t"
        "v_cvt_pk_bf16_f32 v35, v110, v111\n\t"
        "v_cvt_pk_bf16_f32 v36, v112, v113\n\t"
        "v_cvt_pk_bf16_f32 v37, v114, v115\n\t"
        "v_cvt_pk_bf16_f32 v38, v116, v117\n\t"
        "v_cvt_pk_bf16_f32 v39, v118, v119\n\t"
        "v_cvt_pk_bf16_f32 v40, v120, v121\n\t"
        "v_cvt_pk_bf16_f32 v41, v122, v123\n\t"
        "v_cvt_pk_bf16_f32 v42, v124, v125\n\t"
        "v_cvt_pk_bf16_f32 v43, v126, v127\n\t"
        "s_nop 1\n\t"
        "v_permlane16_swap_b32 v32, v34\n\t"
        "v_permlane16_swap_b32 v33, v35\n\t"
        "v_permlane16_swap_b32 v36, v38\n\t"
        "v_permlane16_swap_b32 v37, v39\n\t"
        "v_permlane16_swap_b32 v40, v42\n\t"
        "v_permlane16_swap_b32 v41, v43\n\t"
        "buffer_store_dwordx4 v[32:35], %[vo], %[ro], 0 offen offset:0 sc1\n\t"
        "buffer_store_dwordx4 v[36:39], %[vo], %[ro], 0 offen offset:64 sc1\n\t"
        "buffer_store_dwordx4 v[40:43], %[vo], %[ro], 0 offen offset:128 sc1\n\t"
        "s_cmp_eq_u32 %[nv], 1\n\t"
        "s_cbranch_scc1 8f\n\t"
        "s_mov_b32 %[tn], 172032\n\t"
        "v_cvt_pk_bf16_f32 v44, v128, v129\n\t"
        "v_cvt_pk_bf16_f32 v45, v130, v131\n\t"
        "v_cvt_pk_bf16_f32 v46, v132, v133\n\t"
        "v_cvt_pk_bf16_f32 v47, v134, v135\n\t"
        "v_cvt_pk_bf16_f32 v48, v136, v137\n\t"
        "v_cvt_pk_bf16_f32 v49, v138, v139\n\t"
        "v_cvt_pk_bf16_f32 v50, v140, v141\n\t"
        "v_cvt_pk_bf16_f32 v51, v142, v143\n\t"
        "v_cvt_pk_bf16_f32 v52, v144, v145\n\t"
        "v_cvt_pk_bf16_f32 v53, v146, v147\n\t"
        "v_cvt_pk_bf16_f32 v54, v148, v149\n\t"
        "v_cvt_pk_bf16_f32 v55, v150, v151\n\t"
        "s_nop 1\n\t"
        "v_permlane16_swap_b32 v44, v46\n\t"
        "v_permlane16_swap_b32 v45, v47\n\t"
        "v_permlane16_swap_b32 v48, v50\n\t"
        "v_permlane16_swap_b32 v49, v51\n\t"
        "v_permlane16_swap_b32 v52, v54\n\t"
        "v_permlane16_swap_b32 v53, v55\n\t"
        "buffer_store_dwordx4 v[44:47], %[vo], %[ro], %[tn] offen offset:0 sc1\n\t"
        "buffer_store_dwordx4 v[48:51], %[vo], %[ro], %[tn] offen offset:64 sc1\n\t"
        "buffer_store_dwordx4 v[52:55], %[vo], %[ro], %[tn] offen offset:128 sc1\n\t"
        "8:\n\t"
                : [ca] "+&v"(ca), [tn] "+&s"(tn)
                : [wa0] "v"(wb), [wa1] "v"(wb ^ 32), [ta] "v"(TBL + wv * 256 + bc.l4 * 16), [cx] "v"((lane & 3) * 0x10001),
                  [sel0] "v"(bc.sel0), [sel1] "v"(bc.sel1), [sel2] "v"(bc.sel2), [sel3] "v"(bc.sel3),
                  [vo] "v"(((ys + wv) * IMG + xs + bc.col) * PXB + ((q & 2) << 3) + ((q & 1) << 5)), [ro] "s"(ro), [nv] "s"((R + 3 - wv) >> 2), [rr] "s"(R)
                : "memory", "vcc", "scc", "v13", "v14", "v15", "v16", "v17", "v18", "v19", "v20", "v21", "v22", "v23", "v24", "v25", "v26", "v27", "v28", "v29", "v30", "v31", "v32", "v33", "v34", "v35", "v36", "v37", "v38", "v39", "v40", "v41", "v42", "v43", "v44", "v45", "v46", "v47", "v48", "v49", "v50", "v51", "v52", "v53", "v54", "v55", "v56", "v57", "v58", "v59", "v60", "v61", "v62", "v63", "v64", "v65", "v66", "v67", "v68", "v69", "v70", "v71", "v72", "v73", "v74", "v75", "v76", "v77", "v78", "v79", "v80", "v81", "v82", "v83", "v84", "v85", "v86", "v87", "v88", "v89", "v90", "v91", "v92", "v93", "v94", "v95", "v96", "v97", "v98", "v99", "v100", "v101", "v102", "v103", "v104", "v105", "v106", "v107", "v108", "v109", "v110", "v111", "v112", "v113", "v114", "v115", "v116", "v117", "v118", "v119", "v120", "v121", "v122", "v123", "v124", "v125", "v126", "v127", "v128", "v129", "v130", "v131", "v132", "v133", "v134", "v135", "v136", "v137", "v138", "v139", "v140", "v141", "v142", "v143", "v144", "v145", "v146", "v147", "v148", "v149", "v150", "v151");
        }
    } else {
        LANE_VARS;
        const u32 sw = wv & 3;
        const bool isB = wv >= 8;
        const bool hasA = !isB && (int)(4 * sw) < R;
        const u32 region = POOL + sw * 6400;
        auto wvo = [&](u32 l) { return (l << 4) ^ ((l / 12 & 8) << 2); };
        auto touch = [&](u32 voff, u32 soff) {
            asm volatile("s_mov_b32 m0, %3\n\ts_nop 0\n\tbuffer_load_dword %0, %1, %2 offen lds" :: "v"(voff), "s"(rw), "s"(soff), "i"(381 * PXB) : "memory");
        };
        auto prefetch_all = [&]() {
            u32 pf = (bid >> 3) * 14400 + lane * 128;
            touch(pf, 0);
            touch(pf, 0x1900);
        };
        if (xr) {
            auto irregular = [&]() { return __builtin_amdgcn_readfirstlane(*LDSP(volatile u32, IRR)); };
            bool bad;
            if (!isB) {
                const __amdgpu_buffer_rsrc_t rf = __builtin_amdgcn_make_buffer_rsrc(
                    (void*)(ptr[2] | (u64)ptr[3] << 32), 0, IMG * IMG * 100, 0x00020000);
                auto slice = [&](u32 k) {
                    const u32x4 rx = make_rsrc(ptr[0], ptr[1], IMG * ROWB);
                    u32 c = wx0 + 10 * (sw & 1) + (lane >> 2), voff = min(c, IMG - 1u) * PXB + (((lane & 3) ^ ((c >> 1) & 2)) << 4) + 64 * k;
                    u32 m0v = k * 18 * 1280 + sw * 640, soff = (wy0 + (sw >> 1)) * ROWB, cnt = 9;
                    asm volatile("s_sleep 12\n\ts_mov_b64 exec, %5\n\t1:\n\ts_mov_b32 m0, %0\n\ts_add_u32 %0, %0, 2560\n\t"
                                 "buffer_load_dwordx4 %3, %4, %1 offen lds\n\ts_add_u32 %1, %1, 86016\n\ts_min_u32 %1, %1, 9590784\n\t"
                                 "s_sub_u32 %2, %2, 1\n\ts_cmp_lg_u32 %2, 0\n\ts_cbranch_scc1 1b\n\ts_mov_b64 exec, -1"
                                 : "+&s"(m0v), "+&s"(soff), "+&s"(cnt) : "v"(voff), "s"(rx), "s"(0xffffffffffull) : "memory", "scc");
                };
                auto slices = [&]() {
                    asm volatile("s_setprio 0\n\ts_barrier" ::: "memory");
                    slice(1);
                    asm volatile("s_barrier" ::: "memory");
                    slice(2);
                    asm volatile("s_barrier" ::: "memory");
                    BARRIER("vmcnt(0)");
                };
                if (xr == 2) {
                    asm volatile("s_barrier" ::: "memory");
                    slices();
                    asm volatile("s_sleep 12" ::: "memory");
                }
                u32x4 o[7];
#pragma unroll
                for (int i = 0; i < 7; ++i) {
                    u32 h = 7 * sw + i;
                    o[i] = __builtin_amdgcn_raw_buffer_load_b128(rf, min(lane, 49u) * 16, ((ys + (h >> 1)) * IMG + xs) * 100 + (h & 1) * 800, 0);
                }
                if (xr == 2) asm volatile("s_barrier" ::: "memory");
                auto theta = [](int b) { return ((__float_as_uint((float)(b - 3)) >> 23) - 8) << 7; };
                const u32 px0 = xs + min(lane, 49u) * 4 / 25, tx0 = theta(px0), tx1 = theta(px0 + 8);
                bool fail = false;
#pragma unroll
                for (int i = 0; i < 7; ++i) {
                    u32 mx = 0, h = 7 * sw + i;
#pragma unroll
                    for (int k = 0; k < 4; ++k) asm("v_pk_maximum3_f16 %0, %0, %1, %1 neg_lo:[0,0,1] neg_hi:[0,0,1]" : "+v"(mx) : "v"(o[i][k]));
                    fail |= (mx & 0xffffu) >= theta(ys + (h >> 1)) || (mx >> 16) >= ((h & 1) ? tx1 : tx0);
                }
                if (fail) *LDSP(u32, IRR) = xr;
                BARRIER("lgkmcnt(0)");
                bad = irregular();
                if (!bad && xr == 1) slices();
            } else {
                if (wv == 11) asm volatile("s_barrier" ::: "memory"); else BARRIER("vmcnt(0)");
                bad = irregular() == 1;
                if (!bad) {
                    if (wv == 8) {
                        asm volatile(
        "s_mov_b32 s40, 104448\n\t"
        "s_movk_i32 s41, 18432\n\t"
        "s_mov_b32 s43, 14\n\t"
        "v_mov_b32 v16, 153716\n\t"
        "1:\n\t"
        "s_mov_b32 s42, s41\n\t"
        "s_mov_b32 s44, s40\n\t"
        "s_mov_b32 s45, 5\n\t"
        "2:\n\t"
        "s_add_u32 m0, s44, 0\n\t"
        "s_nop 0\n\t"
        "buffer_load_dwordx4 %[wv0], %[rw], s42 offen lds\n\t"
        "buffer_load_dwordx4 %[wv0], %[rw], s42 offen offset:3072 lds\n\t"
        "s_add_u32 m0, s44, 1024\n\t"
        "s_nop 0\n\t"
        "buffer_load_dwordx4 %[wv1], %[rw], s42 offen lds\n\t"
        "buffer_load_dwordx4 %[wv1], %[rw], s42 offen offset:3072 lds\n\t"
        "s_add_u32 m0, s44, 2048\n\t"
        "s_nop 0\n\t"
        "buffer_load_dwordx4 %[wv2], %[rw], s42 offen lds\n\t"
        "buffer_load_dwordx4 %[wv2], %[rw], s42 offen offset:3072 lds\n\t"
        "s_add_u32 s42, s42, 92160\n\t"
        "s_add_u32 s44, s44, 6144\n\t"
        "s_sub_u32 s45, s45, 1\n\t"
        "s_cmp_lg_u32 s45, 0\n\t"
        "s_cbranch_scc1 2b\n\t"
        "s_cmp_eq_u32 s43, 8\n\t"
        "s_cbranch_scc1 3f\n\t"
        "4:\n\t"
        "s_xor_b32 s40, s40, 47104\n\t"
        "s_add_u32 s41, s41, 18432\n\t"
        "s_cmp_ge_u32 s41, 92160\n\t"
        "s_cselect_b32 s42, 86016, 0\n\t"
        "s_sub_u32 s41, s41, s42\n\t"
        "s_sub_u32 s43, s43, 1\n\t"
        "s_cmp_lg_u32 s43, 0\n\t"
        "s_waitcnt vmcnt(0)\n\t"
        "s_barrier\n\t"
        "s_cbranch_scc1 1b\n\t"
        "s_endpgm\n\t"
        "3:\n\t"
        "ds_read_b32 v17, v16\n\t"
        "s_waitcnt lgkmcnt(0)\n\t"
        "v_cmp_eq_u32 vcc, 0, v17\n\t"
        "s_cbranch_vccnz 4b\n\t"
                            :: [wv0] "v"(wvo(lane)), [wv1] "v"(wvo(lane + 64)), [wv2] "v"(wvo(lane + 128)), [rw] "s"(rw) : "memory", "scc", "v16", "v17", "vcc", "s40", "s41", "s42", "s43", "s44", "s45");
                        bad = true;
                    } else {
                        asm volatile("s_barrier" ::: "memory");
                        if (wv == 11) prefetch_all();
                        asm volatile("s_barrier\n\ts_barrier\n\ts_barrier\n\ts_barrier\n\ts_barrier" ::: "memory");
                        bad = irregular();
                    }
                }
            }
            if (!bad) asm volatile("s_endpgm");
            asm volatile(
        "s_setprio 0\n\t"
        "s_waitcnt vmcnt(0) lgkmcnt(0)\n\t"
        "s_barrier\n\t"
        "v_mbcnt_lo_u32_b32 v17, -1, 0\n\t"
        "v_mbcnt_hi_u32_b32 v17, -1, v17\n\t"
        "v_lshl_add_u32 v17, %[wv], 6, v17\n\t"
        "s_mov_b64 s[60:61], %[ka]\n\t"
        "s_or_b32 s62, %[bid], 0x100\n\t"
        "s_setpc_b64 %[re]\n\t"
                :: RESTART_IN : "memory", "scc", "v17", "s60", "s61", "s62");
        }
        if (hasA) {
            const u32 tile = 4 * sw + q, hr = (R + 1) >> 1, tm = min(tile < hr ? tile : tile + 8 - hr, 15u);
            const float fx0 = (float)(xs + (int)p - 2), fy = (float)(ys + (int)tile - 2);
            const u64 okm = __builtin_amdgcn_ballot_w64((int)tile < R);
            const float xhi = (float)min(IMG - 1, wx0 + wcols - 1);
#define A_OPERANDS(TW) \
                :: [fy] "v"(fy), [fx0] "v"(fx0), \
                   [whi] "v"(pk_bf16((float)min(IMG - 1, wy0 + wrows - 1), xhi)), [wh1] "v"(pk_bf16((float)min(IMG - 1, wy0 + wrows - 1 - 3), xhi)), \
                   [wlo] "v"(pk_bf16((float)wy0, (float)wx0)), \
                   [p32] "s"(wp * 6), [p32v] "v"(wp * 6), [c32] "s"(-(wy0 * wp + wx0) * 6), \
                   [c32k] "v"(0u - (u32)(wy0 * wp + wx0) * 6u * 0x20002u), [okm] "s"(okm), [tw] "v"(TW), [rw] "s"(rw), \
                   [pf] "v"(sw ? 0x7ff00000u : (bid >> 3) * 14400 + lane * 128), [oa] "v"(region + q * 1600 + p * 100)
            if (!ic) {
                asm volatile(
        "s_setprio 1\n\t"
        "v_mov_b32 v70, %[fy]\n\t"
        "v_mov_b32 v71, %[fx0]\n\t"
        "v_mov_b32 v72, %[wh1]\n\t"
        "v_mov_b32 v73, %[tw]\n\t"
        "v_mov_b32 v74, 153592\n\t"
        "s_mov_b32 s47, 1449555558\n\t"
        "s_mov_b32 s48, 0x20002\n\t"
        "s_movk_i32 s49, 0x3120\n\t"
        "s_mov_b32 s40, 0\n\t"
        "s_mov_b32 s41, 0\n\t"
        "s_mov_b32 s44, 153728\n\t"
        "s_mov_b32 s45, 110592\n\t"
        "s_movk_i32 s46, 192\n\t"
        "s_mov_b64 s[62:63], %[okm]\n\t"
        "s_waitcnt vmcnt(0)\n\t"
        "ds_read2_b32 v[100:101], %[oa] offset0:0 offset1:1\n\t"
        "ds_read2_b32 v[102:103], %[oa] offset0:2 offset1:3\n\t"
        "ds_read2_b32 v[104:105], %[oa] offset0:4 offset1:5\n\t"
        "ds_read2_b32 v[106:107], %[oa] offset0:6 offset1:7\n\t"
        "ds_read2_b32 v[108:109], %[oa] offset0:8 offset1:9\n\t"
        "ds_read2_b32 v[110:111], %[oa] offset0:10 offset1:11\n\t"
        "ds_read2_b32 v[112:113], %[oa] offset0:12 offset1:13\n\t"
        "ds_read2_b32 v[114:115], %[oa] offset0:14 offset1:15\n\t"
        "ds_read2_b32 v[116:117], %[oa] offset0:16 offset1:17\n\t"
        "ds_read2_b32 v[118:119], %[oa] offset0:18 offset1:19\n\t"
        "ds_read2_b32 v[120:121], %[oa] offset0:20 offset1:21\n\t"
        "ds_read2_b32 v[122:123], %[oa] offset0:22 offset1:23\n\t"
        "ds_read_b32 v124, %[oa] offset:96\n\t"
        "s_waitcnt lgkmcnt(11)\n\t"
        "s_branch 2f\n\t"
        ".p2align 6\n\t"
        "1:\n\t"
        "s_waitcnt lgkmcnt(0)\n\t"
        "v_add_u32 v64, v64, v65\n\t"
        "v_cmp_lt_u16 vcc, 208, v64\n\t"
        "s_barrier\n\t"
        "s_cbranch_vccnz 7f\n\t"
        "4:\n\t"
        "2:\n\t"
        "s_set_gpr_idx_on s40, gpr_idx(SRC0,SRC1)\n\t"
        "v_lshlrev_b32 v77, 16, v100\n\t"
        "v_and_b32 v78, 0xffff0000, v100\n\t"
        "s_set_gpr_idx_off\n\t"
        "v_add_f32 v77, v70, v77\n\t"
        "v_add_f32 v78, v71, v78\n\t"
        "v_cvt_pk_bf16_f32 v75, v77, v78\n\t"
        "v_pk_max_i16 v76, v75, %[wlo]\n\t"
        "v_pk_min_i16 v76, v76, v72\n\t"
        "v_and_b32 v79, 0xffff0000, v76\n\t"
        "v_lshlrev_b32 v80, 16, v76\n\t"
        "v_cmp_ne_u32 s[52:53], v75, v76\n\t"
        "v_fract_f32 v81, v79\n\t"
        "v_fract_f32 v82, v80\n\t"
        "v_cmp_neq_f32 s[54:55], 0, v81\n\t"
        "v_cmp_neq_f32 s[56:57], 0, v82\n\t"
        "v_cvt_u32_f32 v83, v79\n\t"
        "v_cvt_u32_f32 v84, v80\n\t"
        "ds_write_b32 v73, v91\n\t"
        "ds_read2_b32 v[64:65], v74 offset1:1\n\t"
        "v_bfe_u32 v77, v83, 2, 1\n\t"
        "v_mad_u32_u24 v77, v83, 6, v77\n\t"
        "s_or_b64 s[60:61], s[54:55], s[56:57]\n\t"
        "s_or_b64 s[58:59], s[60:61], s[52:53]\n\t"
        "v_mad_u32_u24 v92, v84, %[p32], v77\n\t"
        "v_add_lshl_u32 v91, v92, %[c32], 5\n\t"
        "s_and_b64 s[58:59], s[58:59], s[62:63]\n\t"
        "s_cbranch_scc1 8f\n\t"
        "5:\n\t"
        "v_xor_b32 v73, 0x400, v73\n\t"
        "v_add_f32 v71, 1.0, v71\n\t"
        "s_add_u32 s44, s44, 2688\n\t"
        "s_cmp_eq_u32 s44, 161792\n\t"
        "v_add_u32 v74, 4, v74\n\t"
        "s_cselect_b32 s44, 153728, s44\n\t"
        "s_xor_b32 s45, s45, 255808\n\t"
        "s_sub_u32 s46, 0, s46\n\t"
        "s_add_u32 s40, s40, 1\n\t"
        "s_add_u32 s41, s41, 1\n\t"
        "s_cmp_eq_u32 s41, 5\n\t"
        "s_cbranch_scc1 9f\n\t"
        "s_cmp_gt_u32 s40, 3\n\t"
        "s_cbranch_scc1 1b\n\t"
        "s_cmp_lt_u32 s40, 3\n\t"
        "s_cbranch_scc1 2b\n\t"
        "s_waitcnt lgkmcnt(0)\n\t"
        "s_barrier\n\t"
        "v_mov_b32 v77, 153600\n\t"
        "ds_read2_b32 v[64:65], v77 offset1:1\n\t"
        "s_waitcnt lgkmcnt(0)\n\t"
        "v_add_u32 v64, v64, v65\n\t"
        "v_cmp_lt_u16 vcc, 208, v64\n\t"
        "s_barrier\n\t"
        "buffer_load_dword v125, %[pf], %[rw], 0 offen\n\t"
        "s_movk_i32 s42, 0x1900\n\t"
        "buffer_load_dword v125, %[pf], %[rw], s42 offen\n\t"
        "s_cbranch_vccz 4b\n\t"
        "7:\n\t"
        "s_barrier\n\t"
        "s_branch 4b\n\t"
        "9:\n\t"
        "s_mov_b32 s41, 0\n\t"
        "v_mov_b32 v71, %[fx0]\n\t"
        "v_add_f32 v70, 1.0, v70\n\t"
        "v_mov_b32 v72, %[whi]\n\t"
        "s_cmp_lg_u32 s40, 25\n\t"
        "s_cbranch_scc1 1b\n\t"
        "s_waitcnt lgkmcnt(0)\n\t"
        "v_add_u32 v64, v64, v65\n\t"
        "v_cmp_lt_u16 vcc, 208, v64\n\t"
        "s_barrier\n\t"
        "s_cbranch_vccz 20f\n\t"
        "s_barrier\n\t"
        "20:\n\t"
        "ds_read2_b32 v[64:65], v74 offset0:0 offset1:1\n\t"
        "ds_write_b32 v73, v91\n\t"
        "s_waitcnt lgkmcnt(0)\n\t"
        "v_add_u32 v64, v64, v65\n\t"
        "v_cmp_lt_u16 vcc, 208, v64\n\t"
        "s_barrier\n\t"
        "s_cbranch_vccz 21f\n\t"
        "s_barrier\n\t"
        "21:\n\t"
        "ds_read2_b32 v[64:65], v74 offset0:1 offset1:2\n\t"
        "s_waitcnt lgkmcnt(0)\n\t"
        "v_add_u32 v64, v64, v65\n\t"
        "v_cmp_lt_u16 vcc, 208, v64\n\t"
        "s_barrier\n\t"
        "s_cbranch_vccz 22f\n\t"
        "s_barrier\n\t"
        "22:\n\t"
        "s_barrier\n\t"
        "s_endpgm\n\t"
        "10:\n\t"
        "v_pk_max_i16 v90, v75, 0\n\t"
        "s_mov_b32 s42, 0x435f435f\n\t"
        "v_pk_min_i16 v90, v90, s42\n\t"
        "v_cmp_ne_u32 s[52:53], v90, v76\n\t"
        "s_and_b64 s[52:53], s[52:53], s[62:63]\n\t"
        "s_or_b64 s[58:59], s[60:61], s[52:53]\n\t"
        "s_and_b64 s[58:59], s[58:59], s[62:63]\n\t"
        "s_cbranch_scc0 5b\n\t"
        "s_branch 11f\n\t"
        "8:\n\t"
        "s_and_b64 s[52:53], s[52:53], s[62:63]\n\t"
        "s_cbranch_scc1 10b\n\t"
        "11:\n\t"
        "s_bcnt1_i32_b64 s43, s[58:59]\n\t"
        "s_and_b64 s[56:57], s[56:57], s[54:55]\n\t"
        "s_and_b64 s[56:57], s[56:57], s[62:63]\n\t"
        "s_or_b64 s[60:61], s[56:57], s[52:53]\n\t"
        "s_cselect_b32 s42, 0x10000, 0\n\t"
        "s_or_b32 s42, s42, s43\n\t"
        "v_mov_b32 v85, s42\n\t"
        "s_mov_b64 exec, 1\n\t"
        "ds_add_rtn_u32 v86, v74, v85 offset:12\n\t"
        "s_mov_b64 exec, -1\n\t"
        "v_add_f32 v78, v82, v81\n\t"
        "v_lshlrev_b32 v77, 2, v83\n\t"
        "v_sub_f32 v87, 1.0, v78\n\t"
        "v_bfe_u32 v77, s47, v77, 4\n\t"
        "v_cvt_pk_bf16_f32 v67, v87, v78\n\t"
        "v_cndmask_b32 v77, %[p32v], v77, s[54:55]\n\t"
        "v_mad_u32_u24 v92, v92, s48, %[c32k]\n\t"
        "v_lshl_add_u32 v66, v77, 17, v92\n\t"
        "v_mbcnt_lo_u32_b32 v85, s58, 0\n\t"
        "v_mbcnt_hi_u32_b32 v85, s59, v85\n\t"
        "s_cmp_lg_u64 s[60:61], 0\n\t"
        "s_cbranch_scc1 12f\n\t"
        "13:\n\t"
        "s_waitcnt lgkmcnt(0)\n\t"
        "v_readfirstlane_b32 s43, v86\n\t"
        "s_mov_b64 exec, s[58:59]\n\t"
        "s_and_b32 s43, s43, 0xffff\n\t"
        "v_add_u32 v85, s43, v85\n\t"
        "v_lshl_add_u32 v77, v85, 3, s44\n\t"
        "ds_write_b64 v77, v[66:67]\n\t"
        "v_and_b32 v78, 12, v85\n\t"
        "v_bfe_u32 v78, s49, v78, 2\n\t"
        "v_lshl_add_u32 v78, v78, 4, s45\n\t"
        "v_mad_i32_i24 v91, v85, s46, v78\n\t"
        "s_and_b64 exec, s[56:57], s[56:57]\n\t"
        "s_cbranch_scc1 14f\n\t"
        "s_mov_b64 exec, -1\n\t"
        "s_branch 5b\n\t"
        "14:\n\t"
        "v_lshl_add_u32 v77, v85, 2, s44\n\t"
        "ds_write_b32 v77, v93 offset:1792\n\t"
        "s_mov_b64 exec, -1\n\t"
        "s_branch 5b\n\t"
        "12:\n\t"
        "v_sub_f32 v87, 1.0, v82\n\t"
        "v_sub_f32 v88, 1.0, v81\n\t"
        "v_cvt_pk_bf16_f32 v88, v87, v88\n\t"
        "v_lshlrev_b32 v87, 16, v88\n\t"
        "v_and_b32 v88, 0xffff0000, v88\n\t"
        "v_mul_f32 v89, v82, v88\n\t"
        "v_mul_f32 v77, v87, v81\n\t"
        "v_mul_f32 v87, v87, -v88\n\t"
        "v_mul_f32 v78, v82, v81\n\t"
        "v_cvt_pk_bf16_f32 v87, v87, v77\n\t"
        "v_cvt_pk_bf16_f32 v93, v89, v78\n\t"
        "v_cndmask_b32 v67, v67, v87, s[56:57]\n\t"
        "s_cmp_eq_u64 s[52:53], 0\n\t"
        "s_cbranch_scc1 13b\n\t"
        "v_or_b32 v90, 0x80000000, v90\n\t"
        "v_cndmask_b32 v66, v66, 0, s[52:53]\n\t"
        "v_cndmask_b32 v67, v67, v90, s[52:53]\n\t"
        "s_branch 13b\n\t"
                A_OPERANDS(TBL + 1024 + ((tm >> 2) ^ 2) * 256 + p * 16 + (tm & 3) * 4) : "memory", "vcc", "scc", "v64", "v65", "v66", "v67", "v68", "v69", "v70", "v71", "v72", "v73", "v74", "v75", "v76", "v77", "v78", "v79", "v80", "v81", "v82", "v83", "v84", "v85", "v86", "v87", "v88", "v89", "v90", "v91", "v92", "v93", "v94", "v95", "v96", "v97", "v98", "v99", "v100", "v101", "v102", "v103", "v104", "v105", "v106", "v107", "v108", "v109", "v110", "v111", "v112", "v113", "v114", "v115", "v116", "v117", "v118", "v119", "v120", "v121", "v122", "v123", "v124", "v125", "s40", "s41", "s42", "s43", "s44", "s45", "s46", "s47", "s48", "s49", "s50", "s51", "s52", "s53", "s54", "s55", "s56", "s57", "s58", "s59", "s60", "s61", "s62", "s63");
            } else {
                asm volatile(
        "s_setprio 1\n\t"
        "v_mov_b32 v70, %[fy]\n\t"
        "v_mov_b32 v71, %[fx0]\n\t"
        "v_mov_b32 v72, %[wh1]\n\t"
        "v_mov_b32 v73, %[tw]\n\t"
        "v_mov_b32 v74, 153592\n\t"
        "s_mov_b32 s47, 1449555558\n\t"
        "s_mov_b32 s48, 0x20002\n\t"
        "s_movk_i32 s49, 0x3120\n\t"
        "s_mov_b32 s40, 0\n\t"
        "s_mov_b32 s41, 0\n\t"
        "s_mov_b32 s44, 153728\n\t"
        "s_mov_b32 s45, 110592\n\t"
        "s_movk_i32 s46, 192\n\t"
        "s_mov_b64 s[62:63], %[okm]\n\t"
        "s_waitcnt vmcnt(0)\n\t"
        "ds_read2_b32 v[100:101], %[oa] offset0:0 offset1:1\n\t"
        "ds_read2_b32 v[102:103], %[oa] offset0:2 offset1:3\n\t"
        "ds_read2_b32 v[104:105], %[oa] offset0:4 offset1:5\n\t"
        "ds_read2_b32 v[106:107], %[oa] offset0:6 offset1:7\n\t"
        "ds_read2_b32 v[108:109], %[oa] offset0:8 offset1:9\n\t"
        "ds_read2_b32 v[110:111], %[oa] offset0:10 offset1:11\n\t"
        "ds_read2_b32 v[112:113], %[oa] offset0:12 offset1:13\n\t"
        "ds_read2_b32 v[114:115], %[oa] offset0:14 offset1:15\n\t"
        "ds_read2_b32 v[116:117], %[oa] offset0:16 offset1:17\n\t"
        "ds_read2_b32 v[118:119], %[oa] offset0:18 offset1:19\n\t"
        "ds_read2_b32 v[120:121], %[oa] offset0:20 offset1:21\n\t"
        "ds_read2_b32 v[122:123], %[oa] offset0:22 offset1:23\n\t"
        "ds_read_b32 v124, %[oa] offset:96\n\t"
        "s_waitcnt lgkmcnt(11)\n\t"
        "s_branch 2f\n\t"
        ".p2align 6\n\t"
        "1:\n\t"
        "s_waitcnt lgkmcnt(0)\n\t"
        "v_add_u32 v64, v64, v65\n\t"
        "v_cmp_lt_u16 vcc, 208, v64\n\t"
        "s_barrier\n\t"
        "s_cbranch_vccnz 7f\n\t"
        "4:\n\t"
        "2:\n\t"
        "s_set_gpr_idx_on s40, gpr_idx(SRC0,SRC1)\n\t"
        "v_lshlrev_b32 v77, 16, v100\n\t"
        "v_and_b32 v78, 0xffff0000, v100\n\t"
        "s_set_gpr_idx_off\n\t"
        "v_add_f32 v77, v70, v77\n\t"
        "v_add_f32 v78, v71, v78\n\t"
        "v_cvt_pk_bf16_f32 v75, v77, v78\n\t"
        "v_pk_max_i16 v76, v75, %[wlo]\n\t"
        "v_pk_min_i16 v76, v76, v72\n\t"
        "v_and_b32 v79, 0xffff0000, v76\n\t"
        "v_lshlrev_b32 v80, 16, v76\n\t"
        "v_cmp_ne_u32 s[52:53], v75, v76\n\t"
        "v_fract_f32 v81, v79\n\t"
        "v_fract_f32 v82, v80\n\t"
        "v_cmp_neq_f32 s[54:55], 0, v81\n\t"
        "v_cmp_neq_f32 s[56:57], 0, v82\n\t"
        "v_cvt_u32_f32 v83, v79\n\t"
        "v_cvt_u32_f32 v84, v80\n\t"
        "ds_write_b64 v73, v[68:69]\n\t"
        "ds_read2_b32 v[64:65], v74 offset1:1\n\t"
        "v_bfe_u32 v77, v83, 2, 1\n\t"
        "v_mad_u32_u24 v77, v83, 6, v77\n\t"
        "s_and_b64 s[60:61], s[54:55], s[56:57]\n\t"
        "s_or_b64 s[58:59], s[60:61], s[52:53]\n\t"
        "v_mad_u32_u24 v92, v84, %[p32], v77\n\t"
        "v_add_f32 v78, v82, v81\n\t"
        "v_lshlrev_b32 v77, 2, v83\n\t"
        "v_sub_f32 v87, 1.0, v78\n\t"
        "v_bfe_u32 v77, s47, v77, 4\n\t"
        "v_cvt_pk_bf16_f32 v69, v87, v78\n\t"
        "v_cndmask_b32 v78, 0, %[p32v], s[56:57]\n\t"
        "v_cndmask_b32 v77, v78, v77, s[54:55]\n\t"
        "v_mad_u32_u24 v92, v92, s48, %[c32k]\n\t"
        "v_lshl_add_u32 v68, v77, 17, v92\n\t"
        "s_and_b64 s[58:59], s[58:59], s[62:63]\n\t"
        "s_cbranch_scc1 8f\n\t"
        "5:\n\t"
        "s_setprio 1\n\t"
        "v_xor_b32 v73, 0x400, v73\n\t"
        "v_add_f32 v71, 1.0, v71\n\t"
        "s_add_u32 s44, s44, 2688\n\t"
        "s_cmp_eq_u32 s44, 161792\n\t"
        "v_add_u32 v74, 4, v74\n\t"
        "s_cselect_b32 s44, 153728, s44\n\t"
        "s_xor_b32 s45, s45, 255808\n\t"
        "s_sub_u32 s46, 0, s46\n\t"
        "s_add_u32 s40, s40, 1\n\t"
        "s_add_u32 s41, s41, 1\n\t"
        "s_cmp_eq_u32 s41, 5\n\t"
        "s_cbranch_scc1 9f\n\t"
        "s_cmp_gt_u32 s40, 3\n\t"
        "s_cbranch_scc1 1b\n\t"
        "s_cmp_eq_u32 s40, 2\n\t"
        "s_cbranch_scc1 2b\n\t"
        "s_cmp_eq_u32 s40, 3\n\t"
        "s_cbranch_scc1 15f\n\t"
        "ds_write_b64 v73, v[68:69]\n\t"
        "s_waitcnt lgkmcnt(0)\n\t"
        "s_barrier\n\t"
        "s_branch 2b\n\t"
        "15:\n\t"
        "v_mov_b32 v77, 153600\n\t"
        "ds_read2_b32 v[64:65], v77 offset1:1\n\t"
        "s_waitcnt lgkmcnt(0)\n\t"
        "v_add_u32 v64, v64, v65\n\t"
        "v_cmp_lt_u16 vcc, 208, v64\n\t"
        "s_barrier\n\t"
        "buffer_load_dword v125, %[pf], %[rw], 0 offen\n\t"
        "s_movk_i32 s42, 0x1900\n\t"
        "buffer_load_dword v125, %[pf], %[rw], s42 offen\n\t"
        "s_cbranch_vccz 4b\n\t"
        "7:\n\t"
        "s_barrier\n\t"
        "s_branch 4b\n\t"
        "9:\n\t"
        "s_mov_b32 s41, 0\n\t"
        "v_mov_b32 v71, %[fx0]\n\t"
        "v_add_f32 v70, 1.0, v70\n\t"
        "v_mov_b32 v72, %[whi]\n\t"
        "s_cmp_lg_u32 s40, 25\n\t"
        "s_cbranch_scc1 1b\n\t"
        "s_waitcnt lgkmcnt(0)\n\t"
        "v_add_u32 v64, v64, v65\n\t"
        "v_cmp_lt_u16 vcc, 208, v64\n\t"
        "s_barrier\n\t"
        "s_cbranch_vccz 20f\n\t"
        "s_barrier\n\t"
        "20:\n\t"
        "ds_read2_b32 v[64:65], v74 offset0:0 offset1:1\n\t"
        "ds_write_b64 v73, v[68:69]\n\t"
        "s_waitcnt lgkmcnt(0)\n\t"
        "v_add_u32 v64, v64, v65\n\t"
        "v_cmp_lt_u16 vcc, 208, v64\n\t"
        "s_barrier\n\t"
        "s_cbranch_vccz 21f\n\t"
        "s_barrier\n\t"
        "21:\n\t"
        "ds_read2_b32 v[64:65], v74 offset0:1 offset1:2\n\t"
        "s_waitcnt lgkmcnt(0)\n\t"
        "v_add_u32 v64, v64, v65\n\t"
        "v_cmp_lt_u16 vcc, 208, v64\n\t"
        "s_barrier\n\t"
        "s_cbranch_vccz 22f\n\t"
        "s_barrier\n\t"
        "22:\n\t"
        "s_barrier\n\t"
        "s_endpgm\n\t"
        "10:\n\t"
        "v_pk_max_i16 v90, v75, 0\n\t"
        "s_mov_b32 s42, 0x435f435f\n\t"
        "v_pk_min_i16 v90, v90, s42\n\t"
        "v_cmp_ne_u32 s[52:53], v90, v76\n\t"
        "s_and_b64 s[52:53], s[52:53], s[62:63]\n\t"
        "s_or_b64 s[58:59], s[60:61], s[52:53]\n\t"
        "s_and_b64 s[58:59], s[58:59], s[62:63]\n\t"
        "s_cbranch_scc0 5b\n\t"
        "s_branch 11f\n\t"
        "8:\n\t"
        "s_and_b64 s[52:53], s[52:53], s[62:63]\n\t"
        "s_cbranch_scc1 10b\n\t"
        "11:\n\t"
        "s_setprio 3\n\t"
        "s_bcnt1_i32_b64 s43, s[58:59]\n\t"
        "s_and_b64 s[56:57], s[56:57], s[54:55]\n\t"
        "s_and_b64 s[56:57], s[56:57], s[62:63]\n\t"
        "s_or_b64 s[60:61], s[56:57], s[52:53]\n\t"
        "s_cmp_lg_u64 s[52:53], 0\n\t"
        "s_cselect_b32 s42, 0x10000, 0\n\t"
        "s_or_b32 s42, s42, s43\n\t"
        "v_mov_b32 v85, s42\n\t"
        "s_mov_b64 exec, 1\n\t"
        "ds_add_rtn_u32 v86, v74, v85 offset:12\n\t"
        "s_mov_b64 exec, -1\n\t"
        "v_mov_b32 v66, v68\n\t"
        "v_mbcnt_lo_u32_b32 v85, s58, 0\n\t"
        "v_mbcnt_hi_u32_b32 v85, s59, v85\n\t"
        "s_cmp_lg_u64 s[60:61], 0\n\t"
        "s_cbranch_scc1 12f\n\t"
        "13:\n\t"
        "s_waitcnt lgkmcnt(0)\n\t"
        "v_readfirstlane_b32 s43, v86\n\t"
        "s_mov_b64 exec, s[58:59]\n\t"
        "s_and_b32 s43, s43, 0xffff\n\t"
        "v_add_u32 v85, s43, v85\n\t"
        "v_lshl_add_u32 v77, v85, 3, s44\n\t"
        "ds_write_b64 v77, v[66:67]\n\t"
        "v_and_b32 v78, 12, v85\n\t"
        "v_bfe_u32 v78, s49, v78, 2\n\t"
        "v_lshl_add_u32 v78, v78, 4, s45\n\t"
        "v_mad_i32_i24 v91, v85, s46, v78\n\t"
        "v_lshrrev_b32 v91, 4, v91\n\t"
        "v_lshl_or_b32 v68, v91, 16, v91\n\t"
        "v_mov_b32 v69, 0x3f80\n\t"
        "s_and_b64 exec, s[56:57], s[56:57]\n\t"
        "s_cbranch_scc1 14f\n\t"
        "s_mov_b64 exec, -1\n\t"
        "s_branch 5b\n\t"
        "14:\n\t"
        "v_lshl_add_u32 v77, v85, 2, s44\n\t"
        "ds_write_b32 v77, v93 offset:1792\n\t"
        "s_mov_b64 exec, -1\n\t"
        "s_branch 5b\n\t"
        "12:\n\t"
        "v_sub_f32 v87, 1.0, v82\n\t"
        "v_sub_f32 v88, 1.0, v81\n\t"
        "v_cvt_pk_bf16_f32 v88, v87, v88\n\t"
        "v_lshlrev_b32 v87, 16, v88\n\t"
        "v_and_b32 v88, 0xffff0000, v88\n\t"
        "v_mul_f32 v89, v82, v88\n\t"
        "v_mul_f32 v77, v87, v81\n\t"
        "v_mul_f32 v87, v87, -v88\n\t"
        "v_mul_f32 v78, v82, v81\n\t"
        "v_cvt_pk_bf16_f32 v87, v87, v77\n\t"
        "v_cvt_pk_bf16_f32 v93, v89, v78\n\t"
        "v_cndmask_b32 v67, v67, v87, s[56:57]\n\t"
        "s_cmp_eq_u64 s[52:53], 0\n\t"
        "s_cbranch_scc1 13b\n\t"
        "v_or_b32 v90, 0x80000000, v90\n\t"
        "v_cndmask_b32 v66, v66, 0, s[52:53]\n\t"
        "v_cndmask_b32 v67, v67, v90, s[52:53]\n\t"
        "s_branch 13b\n\t"
                A_OPERANDS(TBL + 1024 + lane * 16 + sw * 8) : "memory", "vcc", "scc", "v64", "v65", "v66", "v67", "v68", "v69", "v70", "v71", "v72", "v73", "v74", "v75", "v76", "v77", "v78", "v79", "v80", "v81", "v82", "v83", "v84", "v85", "v86", "v87", "v88", "v89", "v90", "v91", "v92", "v93", "v94", "v95", "v96", "v97", "v98", "v99", "v100", "v101", "v102", "v103", "v104", "v105", "v106", "v107", "v108", "v109", "v110", "v111", "v112", "v113", "v114", "v115", "v116", "v117", "v118", "v119", "v120", "v121", "v122", "v123", "v124", "v125", "s40", "s41", "s42", "s43", "s44", "s45", "s46", "s47", "s48", "s49", "s50", "s51", "s52", "s53", "s54", "s55", "s56", "s57", "s58", "s59", "s60", "s61", "s62", "s63");
            }
        } else if (wv == (R <= 12 ? 7 : 8)) {
            asm volatile(
        "s_setprio 1\n\t"
        "s_mov_b32 s42, 73728\n\t"
        "s_mov_b32 s40, %[so0]\n\t"
        "s_mov_b32 s41, 0\n\t"
        "v_mov_b32 v66, 153600\n\t"
        "s_branch 2f\n\t"
        ".p2align 6\n\t"
        "1:\n\t"
        "s_waitcnt lgkmcnt(0)\n\t"
        "v_add_u32 v64, v64, v65\n\t"
        "v_cmp_lt_u16 vcc, 208, v64\n\t"
        "v_add_u32 v66, 4, v66\n\t"
        "s_waitcnt vmcnt(0)\n\t"
        "s_barrier\n\t"
        "s_cbranch_vccnz 7f\n\t"
        "4:\n\t"
        "2:\n\t"
        "s_cmp_ge_u32 s40, 460800\n\t"
        "s_cbranch_scc1 9f\n\t"
        "s_add_u32 m0, s42, 0\n\t"
        "s_nop 0\n\t"
        "buffer_load_dwordx4 %[wv0], %[rw], s40 offen lds\n\t"
        "s_nop 5\n\t"
        "buffer_load_dwordx4 %[wv0], %[rw], s40 offen offset:3072 lds\n\t"
        "s_add_u32 m0, s42, 1024\n\t"
        "s_nop 4\n\t"
        "buffer_load_dwordx4 %[wv1], %[rw], s40 offen lds\n\t"
        "s_nop 5\n\t"
        "buffer_load_dwordx4 %[wv1], %[rw], s40 offen offset:3072 lds\n\t"
        "s_add_u32 m0, s42, 2048\n\t"
        "s_nop 4\n\t"
        "buffer_load_dwordx4 %[wv2], %[rw], s40 offen lds\n\t"
        "s_nop 5\n\t"
        "buffer_load_dwordx4 %[wv2], %[rw], s40 offen offset:3072 lds\n\t"
        "s_add_u32 s40, s40, 0x1800\n\t"
        "s_cmp_eq_u32 s41, 0\n\t"
        "s_cbranch_scc1 8f\n\t"
        "3:\n\t"
        "s_cmp_ge_u32 s40, 460800\n\t"
        "s_cbranch_scc1 9f\n\t"
        "s_add_u32 m0, s42, 6144\n\t"
        "s_nop 3\n\t"
        "buffer_load_dwordx4 %[wv0], %[rw], s40 offen lds\n\t"
        "s_nop 5\n\t"
        "buffer_load_dwordx4 %[wv0], %[rw], s40 offen offset:3072 lds\n\t"
        "s_add_u32 m0, s42, 7168\n\t"
        "s_nop 4\n\t"
        "buffer_load_dwordx4 %[wv1], %[rw], s40 offen lds\n\t"
        "s_nop 5\n\t"
        "buffer_load_dwordx4 %[wv1], %[rw], s40 offen offset:3072 lds\n\t"
        "s_add_u32 m0, s42, 8192\n\t"
        "s_nop 4\n\t"
        "buffer_load_dwordx4 %[wv2], %[rw], s40 offen lds\n\t"
        "s_nop 5\n\t"
        "buffer_load_dwordx4 %[wv2], %[rw], s40 offen offset:3072 lds\n\t"
        "s_add_u32 s40, s40, 0x1800\n\t"
        "s_cmp_ge_u32 s40, 460800\n\t"
        "s_cbranch_scc1 9f\n\t"
        "s_add_u32 m0, s42, 12288\n\t"
        "s_nop 3\n\t"
        "buffer_load_dwordx4 %[wv0], %[rw], s40 offen lds\n\t"
        "s_nop 5\n\t"
        "buffer_load_dwordx4 %[wv0], %[rw], s40 offen offset:3072 lds\n\t"
        "s_add_u32 m0, s42, 13312\n\t"
        "s_nop 4\n\t"
        "buffer_load_dwordx4 %[wv1], %[rw], s40 offen lds\n\t"
        "s_nop 5\n\t"
        "buffer_load_dwordx4 %[wv1], %[rw], s40 offen offset:3072 lds\n\t"
        "s_add_u32 m0, s42, 14336\n\t"
        "s_nop 4\n\t"
        "buffer_load_dwordx4 %[wv2], %[rw], s40 offen lds\n\t"
        "s_nop 5\n\t"
        "buffer_load_dwordx4 %[wv2], %[rw], s40 offen offset:3072 lds\n\t"
        "s_add_u32 s40, s40, 0x1800\n\t"
        "9:\n\t"
        "ds_read2_b32 v[64:65], v66 offset1:1\n\t"
        "s_xor_b32 s42, s42, 18432\n\t"
        "s_add_u32 s41, s41, 1\n\t"
        "s_cmp_lg_u32 s41, 25\n\t"
        "s_cbranch_scc1 1b\n\t"
        "s_waitcnt vmcnt(0) lgkmcnt(0)\n\t"
        "s_barrier\n\t"
        "s_endpgm\n\t"
        "7:\n\t"
        "s_barrier\n\t"
        "s_branch 4b\n\t"
        "8:\n\t"
        "s_waitcnt vmcnt(12)\n\t"
        "s_barrier\n\t"
        "ds_read2_b32 v[64:65], v66 offset1:1\n\t"
        "s_waitcnt lgkmcnt(0)\n\t"
        "v_add_u32 v64, v64, v65\n\t"
        "v_cmp_lt_u16 vcc, 208, v64\n\t"
        "v_add_u32 v66, 4, v66\n\t"
        "s_waitcnt vmcnt(6)\n\t"
        "s_barrier\n\t"
        "s_cbranch_vccz 3b\n\t"
        "s_barrier\n\t"
        "s_branch 3b\n\t"
                :: [wv0] "v"(wvo(lane)), [wv1] "v"(wvo(lane + 64)), [wv2] "v"(wvo(lane + 128)), [rw] "s"(rw), [so0] "s"(ic ? 2 * 6144 : 4 * 6144)
                : "memory", "vcc", "scc", "v64", "v65", "v66", "s40", "s41", "s42");
        } else {
            const BatchCtx bc = batch_ctx(lane, pitch);
            const int nwork = 7 - ((R + 3) >> 2);
            const int wid = (isB ? 3 : 6) - (int)sw;
            const u32 l60 = min(lane, 59u), pos = 5 * wid + l60 / 12, ch = l60 % 12;
            const u32 de0 = (slot_addr(0, pos) ^ ((ch & 3) << 4)) + 64 * (ch >> 2), de1 = (slot_addr(1, pos) ^ ((ch & 3) << 4)) + 64 * (ch >> 2);
            const int nw16 = to_sgpr(5 * nwork), w16 = to_sgpr(5 * wid);
            int u = 0, thr = NS, fst = 1;
#pragma unroll 1
            for (;;) {
#define B_OPERANDS \
                    : [u] "+&s"(u), [thr] "+&s"(thr), [fst] "+&s"(fst) \
                    : [lo] "s"(u % 3 * LSZ), [c16] "v"(64 * (ch >> 2)), [cx] "v"((ch & 3) * 0x10001), [la0] "v"(LIST + pos * 8), \
                      [ly0] "v"(LIST + NS * 8 + pos * 4), [de0] "v"(de0), [de1] "v"(de1), \
                      [nw16] "s"(nw16), [w16] "s"(w16)
                if (!ic) {
                    asm volatile(
        "s_setprio 1\n\t"
        "s_mov_b32 s45, 0xffff0000\n\t"
        "s_mov_b32 s48, 0x07060302\n\t"
        "s_sub_u32 %[u], %[u], 1\n\t"
        "s_bitcmp1_b32 %[u], 0\n\t"
        "s_cselect_b64 s[46:47], -1, 0\n\t"
        "s_lshl2_add_u32 s40, %[u], 153608\n\t"
        "v_mov_b32 v44, s40\n\t"
        "s_mov_b32 s42, %[lo]\n\t"
        "v_add_u32 v45, s42, %[la0]\n\t"
        "s_mov_b32 s44, 0\n\t"
        "s_sub_u32 s41, 208, %[thr]\n\t"
        "s_cmp_eq_u32 %[fst], 0\n\t"
        "s_cbranch_scc1 20f\n\t"
        "s_waitcnt vmcnt(6)\n\t"
        "s_barrier\n\t"
        "20:\n\t"
        "ds_read_b32 v43, v44\n\t"
        "ds_read_b64 v[40:41], v45\n\t"
        "s_waitcnt lgkmcnt(0)\n\t"
        "s_branch 6f\n\t"
        ".p2align 6\n\t"
        "1:\n\t"
        "s_barrier\n\t"
        "2:\n\t"
        "s_cmp_eq_u32 s43, 1\n\t"
        "s_cbranch_scc0 5f\n\t"
        "s_waitcnt lgkmcnt(0)\n\t"
        "v_lshlrev_b32 v52, 16, v60\n\t"
        "v_and_b32 v53, s45, v60\n\t"
        "v_mul_f32 v50, v54, v52\n\t"
        "v_mul_f32 v51, v54, v53\n\t"
        "v_lshlrev_b32 v52, 16, v64\n\t"
        "v_and_b32 v53, s45, v64\n\t"
        "v_fmac_f32 v50, v55, v52\n\t"
        "v_fmac_f32 v51, v55, v53\n\t"
        "v_cvt_pk_bf16_f32 v76, v50, v51\n\t"
        "v_lshlrev_b32 v52, 16, v61\n\t"
        "v_and_b32 v53, s45, v61\n\t"
        "v_mul_f32 v50, v54, v52\n\t"
        "v_mul_f32 v51, v54, v53\n\t"
        "v_lshlrev_b32 v52, 16, v65\n\t"
        "v_and_b32 v53, s45, v65\n\t"
        "v_fmac_f32 v50, v55, v52\n\t"
        "v_fmac_f32 v51, v55, v53\n\t"
        "v_cvt_pk_bf16_f32 v77, v50, v51\n\t"
        "ds_read_b32 v43, v44\n\t"
        "ds_read_b64 v[40:41], v45\n\t"
        "v_lshlrev_b32 v52, 16, v62\n\t"
        "v_and_b32 v53, s45, v62\n\t"
        "v_mul_f32 v50, v54, v52\n\t"
        "v_mul_f32 v51, v54, v53\n\t"
        "v_lshlrev_b32 v52, 16, v66\n\t"
        "v_and_b32 v53, s45, v66\n\t"
        "v_fmac_f32 v50, v55, v52\n\t"
        "v_fmac_f32 v51, v55, v53\n\t"
        "v_cvt_pk_bf16_f32 v78, v50, v51\n\t"
        "v_lshlrev_b32 v52, 16, v63\n\t"
        "v_and_b32 v53, s45, v63\n\t"
        "v_mul_f32 v50, v54, v52\n\t"
        "v_mul_f32 v51, v54, v53\n\t"
        "v_lshlrev_b32 v52, 16, v67\n\t"
        "v_and_b32 v53, s45, v67\n\t"
        "v_fmac_f32 v50, v55, v52\n\t"
        "v_fmac_f32 v51, v55, v53\n\t"
        "v_cvt_pk_bf16_f32 v79, v50, v51\n\t"
        "ds_write_b128 v49, v[76:79]\n\t"
        "s_waitcnt lgkmcnt(1)\n\t"
        "6:\n\t"
        "s_sub_u32 %[thr], 208, s41\n\t"
        "v_readfirstlane_b32 s41, v43\n\t"
        "s_cmp_eq_u32 %[u], 0\n\t"
        "s_cbranch_scc1 23f\n\t"
        "24:\n\t"
        "s_min_i32 s40, %[thr], %[nw16]\n\t"
        "s_not_b64 s[46:47], s[46:47]\n\t"
        "s_add_u32 %[u], %[u], 1\n\t"
        "s_cmp_gt_i32 s41, s40\n\t"
        "s_cbranch_scc1 11f\n\t"
        "s_cmp_gt_i32 s41, %[w16]\n\t"
        "s_cselect_b32 s43, 1, 0\n\t"
        "s_cbranch_scc0 12f\n\t"
        "v_xor_b32 v40, %[cx], v40\n\t"
        "v_mad_u32_u16 v47, v40, 16, %[c16]\n\t"
        "v_mad_u32_u16 v48, v40, 16, %[c16] op_sel:[1,0,0,0]\n\t"
        "s_cmp_lg_u32 %[fst], 0\n\t"
        "s_cbranch_scc1 21f\n\t"
        "22:\n\t"
        "ds_read_b128 v[60:63], v47 offset:0\n\t"
        "ds_read_b128 v[64:67], v48 offset:0\n\t"
        "v_cndmask_b32 v49, %[de0], %[de1], s[46:47]\n\t"
        "v_lshlrev_b32 v54, 16, v41\n\t"
        "v_and_b32 v55, s45, v41\n\t"
        "s_waitcnt lgkmcnt(2)\n\t"
        "19:\n\t"
        "s_add_u32 s42, s42, 2688\n\t"
        "s_cmp_eq_u32 s42, 8064\n\t"
        "s_cselect_b32 s42, 0, s42\n\t"
        "v_add_u32 v44, 4, v44\n\t"
        "v_add_u32 v45, s42, %[la0]\n\t"
        "s_cmp_lg_u32 s44, 0\n\t"
        "s_cbranch_scc1 1b\n\t"
        "s_or_b32 s44, s41, %[u]\n\t"
        "s_branch 2b\n\t"
        "5:\n\t"
        "s_cmp_eq_u32 s43, 2\n\t"
        "s_cbranch_scc1 9f\n\t"
        "s_cmp_eq_u32 %[u], 26\n\t"
        "s_cbranch_scc1 9f\n\t"
        "ds_read_b32 v43, v44\n\t"
        "s_waitcnt lgkmcnt(0)\n\t"
        "v_cmp_lt_u16 vcc, %[w16], v43\n\t"
        "s_cbranch_vccz 6b\n\t"
        "ds_read_b64 v[40:41], v45\n\t"
        "s_waitcnt lgkmcnt(0)\n\t"
        "s_branch 6b\n\t"
        "11:\n\t"
        "s_mov_b32 s43, 2\n\t"
        "12:\n\t"
        "s_waitcnt lgkmcnt(0)\n\t"
        "s_cmp_eq_u32 %[fst], 0\n\t"
        "s_cbranch_scc1 19b\n\t"
        "s_mov_b32 %[fst], 0\n\t"
        "s_waitcnt vmcnt(0)\n\t"
        "s_barrier\n\t"
        "s_branch 19b\n\t"
        "21:\n\t"
        "s_mov_b32 %[fst], 0\n\t"
        "s_waitcnt vmcnt(0)\n\t"
        "s_barrier\n\t"
        "s_branch 22b\n\t"
        "23:\n\t"
        "s_cmp_eq_u32 s44, 0\n\t"
        "s_cbranch_scc1 24b\n\t"
        "s_mov_b32 s44, 0\n\t"
        "s_waitcnt lgkmcnt(0)\n\t"
        "s_barrier\n\t"
        "s_branch 24b\n\t"
        "9:\n\t"
                    B_OPERANDS : "memory", "vcc", "scc", "v40", "v41", "v42", "v43", "v44", "v45", "v46", "v47", "v48", "v49", "v50", "v51", "v52", "v53", "v54", "v55", "v56", "v57", "v58", "v59", "v60", "v61", "v62", "v63", "v64", "v65", "v66", "v67", "v68", "v69", "v70", "v71", "v72", "v73", "v74", "v75", "v76", "v77", "v78", "v79", "v80", "v81", "v82", "v83", "v84", "v85", "v86", "v87", "v88", "v89", "v90", "v91", "v92", "v93", "v94", "v95", "s40", "s41", "s42", "s43", "s44", "s45", "s46", "s47", "s48");
                } else {
                    asm volatile(
        "s_setprio 1\n\t"
        "s_mov_b32 s45, 0x05040100\n\t"
        "s_mov_b32 s48, 0x07060302\n\t"
        "s_sub_u32 %[u], %[u], 1\n\t"
        "s_bitcmp1_b32 %[u], 0\n\t"
        "s_cselect_b64 s[46:47], -1, 0\n\t"
        "s_lshl2_add_u32 s40, %[u], 153608\n\t"
        "v_mov_b32 v44, s40\n\t"
        "s_mov_b32 s42, %[lo]\n\t"
        "v_add_u32 v45, s42, %[la0]\n\t"
        "v_add_u32 v46, s42, %[ly0]\n\t"
        "s_mov_b32 s44, 0\n\t"
        "s_sub_u32 s41, 208, %[thr]\n\t"
        "s_cmp_eq_u32 %[fst], 0\n\t"
        "s_cbranch_scc1 20f\n\t"
        "s_waitcnt vmcnt(2)\n\t"
        "s_barrier\n\t"
        "20:\n\t"
        "ds_read_b32 v43, v44\n\t"
        "ds_read_b64 v[40:41], v45\n\t"
        "ds_read_b32 v42, v46\n\t"
        "s_waitcnt lgkmcnt(0)\n\t"
        "s_branch 6f\n\t"
        ".p2align 6\n\t"
        "1:\n\t"
        "s_barrier\n\t"
        "2:\n\t"
        "s_cmp_eq_u32 s43, 1\n\t"
        "s_cbranch_scc0 5f\n\t"
        "s_waitcnt lgkmcnt(0)\n\t"
        "v_perm_b32 v80, v64, v60, s45\n\t"
        "v_perm_b32 v81, v64, v60, s48\n\t"
        "v_perm_b32 v82, v65, v61, s45\n\t"
        "v_perm_b32 v83, v65, v61, s48\n\t"
        "v_perm_b32 v84, v66, v62, s45\n\t"
        "v_perm_b32 v85, v66, v62, s48\n\t"
        "v_perm_b32 v86, v67, v63, s45\n\t"
        "v_perm_b32 v87, v67, v63, s48\n\t"
        "v_perm_b32 v88, v72, v68, s45\n\t"
        "v_perm_b32 v89, v72, v68, s48\n\t"
        "v_perm_b32 v90, v73, v69, s45\n\t"
        "v_perm_b32 v91, v73, v69, s48\n\t"
        "v_perm_b32 v92, v74, v70, s45\n\t"
        "v_perm_b32 v93, v74, v70, s48\n\t"
        "v_perm_b32 v94, v75, v71, s45\n\t"
        "v_perm_b32 v95, v75, v71, s48\n\t"
        "s_setprio 3\n\t"
        "v_dot2_f32_bf16 v80, v80, v41, 0\n\t"
        "v_dot2_f32_bf16 v81, v81, v41, 0\n\t"
        "v_dot2_f32_bf16 v82, v82, v41, 0\n\t"
        "v_dot2_f32_bf16 v83, v83, v41, 0\n\t"
        "v_dot2_f32_bf16 v84, v84, v41, 0\n\t"
        "v_dot2_f32_bf16 v85, v85, v41, 0\n\t"
        "v_dot2_f32_bf16 v86, v86, v41, 0\n\t"
        "v_dot2_f32_bf16 v87, v87, v41, 0\n\t"
        "v_dot2_f32_bf16 v80, v88, v42, v80\n\t"
        "v_dot2_f32_bf16 v81, v89, v42, v81\n\t"
        "v_dot2_f32_bf16 v82, v90, v42, v82\n\t"
        "v_dot2_f32_bf16 v83, v91, v42, v83\n\t"
        "v_dot2_f32_bf16 v84, v92, v42, v84\n\t"
        "v_dot2_f32_bf16 v85, v93, v42, v85\n\t"
        "v_dot2_f32_bf16 v86, v94, v42, v86\n\t"
        "v_dot2_f32_bf16 v87, v95, v42, v87\n\t"
        "s_setprio 1\n\t"
        "ds_read_b32 v43, v44\n\t"
        "ds_read_b64 v[40:41], v45\n\t"
        "ds_read_b32 v42, v46\n\t"
        "v_cvt_pk_bf16_f32 v76, v80, v81\n\t"
        "v_cvt_pk_bf16_f32 v77, v82, v83\n\t"
        "v_cvt_pk_bf16_f32 v78, v84, v85\n\t"
        "v_cvt_pk_bf16_f32 v79, v86, v87\n\t"
        "ds_write_b128 v49, v[76:79]\n\t"
        "s_waitcnt lgkmcnt(1)\n\t"
        "6:\n\t"
        "s_sub_u32 %[thr], 208, s41\n\t"
        "v_readfirstlane_b32 s41, v43\n\t"
        "s_cmp_eq_u32 %[u], 0\n\t"
        "s_cbranch_scc1 23f\n\t"
        "24:\n\t"
        "s_min_i32 s40, %[thr], %[nw16]\n\t"
        "s_not_b64 s[46:47], s[46:47]\n\t"
        "s_add_u32 %[u], %[u], 1\n\t"
        "s_cmp_gt_i32 s41, s40\n\t"
        "s_cbranch_scc1 11f\n\t"
        "s_cmp_gt_i32 s41, %[w16]\n\t"
        "s_cselect_b32 s43, 1, 0\n\t"
        "s_cbranch_scc0 12f\n\t"
        "v_xor_b32 v40, %[cx], v40\n\t"
        "v_mad_u32_u16 v47, v40, 16, %[c16]\n\t"
        "v_mad_u32_u16 v48, v40, 16, %[c16] op_sel:[1,0,0,0]\n\t"
        "s_cmp_lg_u32 %[fst], 0\n\t"
        "s_cbranch_scc1 21f\n\t"
        "22:\n\t"
        "ds_read_b128 v[60:63], v47 offset:0\n\t"
        "ds_read_b128 v[64:67], v48 offset:0\n\t"
        "ds_read_b128 v[68:71], v47 offset:4608\n\t"
        "ds_read_b128 v[72:75], v48 offset:4608\n\t"
        "v_cndmask_b32 v49, %[de0], %[de1], s[46:47]\n\t"
        "v_and_b32 v41, 0x7fff7fff, v41\n\t"
        "s_waitcnt lgkmcnt(4)\n\t"
        "19:\n\t"
        "s_add_u32 s42, s42, 2688\n\t"
        "s_cmp_eq_u32 s42, 8064\n\t"
        "s_cselect_b32 s42, 0, s42\n\t"
        "v_add_u32 v44, 4, v44\n\t"
        "v_add_u32 v45, s42, %[la0]\n\t"
        "v_add_u32 v46, s42, %[ly0]\n\t"
        "s_cmp_lg_u32 s44, 0\n\t"
        "s_cbranch_scc1 1b\n\t"
        "s_or_b32 s44, s41, %[u]\n\t"
        "s_branch 2b\n\t"
        "5:\n\t"
        "s_cmp_eq_u32 s43, 2\n\t"
        "s_cbranch_scc1 9f\n\t"
        "s_cmp_eq_u32 %[u], 26\n\t"
        "s_cbranch_scc1 9f\n\t"
        "ds_read_b32 v43, v44\n\t"
        "s_waitcnt lgkmcnt(0)\n\t"
        "v_cmp_lt_u16 vcc, %[w16], v43\n\t"
        "s_cbranch_vccz 6b\n\t"
        "ds_read_b64 v[40:41], v45\n\t"
        "ds_read_b32 v42, v46\n\t"
        "s_waitcnt lgkmcnt(0)\n\t"
        "s_branch 6b\n\t"
        "11:\n\t"
        "s_mov_b32 s43, 2\n\t"
        "12:\n\t"
        "s_waitcnt lgkmcnt(0)\n\t"
        "s_cmp_eq_u32 %[fst], 0\n\t"
        "s_cbranch_scc1 19b\n\t"
        "s_mov_b32 %[fst], 0\n\t"
        "s_waitcnt vmcnt(0)\n\t"
        "s_barrier\n\t"
        "s_branch 19b\n\t"
        "21:\n\t"
        "s_mov_b32 %[fst], 0\n\t"
        "s_waitcnt vmcnt(0)\n\t"
        "s_barrier\n\t"
        "s_branch 22b\n\t"
        "23:\n\t"
        "s_cmp_eq_u32 s44, 0\n\t"
        "s_cbranch_scc1 24b\n\t"
        "s_mov_b32 s44, 0\n\t"
        "s_waitcnt lgkmcnt(0)\n\t"
        "s_barrier\n\t"
        "s_branch 24b\n\t"
        "9:\n\t"
                    B_OPERANDS : "memory", "vcc", "scc", "v40", "v41", "v42", "v43", "v44", "v45", "v46", "v47", "v48", "v49", "v50", "v51", "v52", "v53", "v54", "v55", "v56", "v57", "v58", "v59", "v60", "v61", "v62", "v63", "v64", "v65", "v66", "v67", "v68", "v69", "v70", "v71", "v72", "v73", "v74", "v75", "v76", "v77", "v78", "v79", "v80", "v81", "v82", "v83", "v84", "v85", "v86", "v87", "v88", "v89", "v90", "v91", "v92", "v93", "v94", "v95", "s40", "s41", "s42", "s43", "s44", "s45", "s46", "s47", "s48");
                }
                if (u == 26) break;
                int n = __builtin_amdgcn_readfirstlane(*LDSP(volatile u32, CNT + 4 * u)) & 0xffff, n1 = min(n, (thr + 16) & ~15);
                int b0 = wid, b1 = (n1 + 15) >> 4;
#pragma unroll 1
                for (int pass = 0; pass < 2; ++pass) {
                    run_batches(b0, b1, nwork, n, u & 1, LIST + u % 3 * LSZ, bc, (const char*)(const __attribute__((address_space(1))) char*)(ptr[0] | (u64)ptr[1] << 32));
                    BARRIER("lgkmcnt(0)");
                    if (n <= thr) break;
                    b0 = b1 + wid; b1 = (n + 15) >> 4;
                }
                thr = NS - 16 - n;
                ++u;
            }
        }
    }
}

extern "C" void kernel_launch(void* const* d_in, const int* in_sizes, int n_in, void* d_out, int out_size,
                              void* d_ws, size_t ws_size, hipStream_t stream) {
    dcn_kernel<<<256, 768, 0, stream>>>((const char*)d_in[0], (const char*)d_in[1], (const char*)d_in[2], (char*)d_out);
}
```
